```python
import torch

def _softmax(x, axis=-1):
    return torch.softmax(x, dim=axis)
def _gelu(x):
    return torch.nn.functional.gelu(x, approximate="tanh")
def _silu(x):
    return torch.nn.functional.silu(x)
def _sigmoid(x):
    return torch.sigmoid(x)
def _layernorm(x, axis=-1, eps=1e-5):
    m = x.mean(dim=axis, keepdim=True)
    v = x.var(dim=axis, keepdim=True, unbiased=False)
    return (x - m) / torch.sqrt(v + eps)
def _scatter_add(tgt, idx, src, axis=0):
    return tgt.clone().index_add_(axis, idx, src)
def _segment_sum(src, seg, n):
    out = torch.zeros((n,) + src.shape[1:], dtype=src.dtype)
    return out.index_add_(0, seg, src)
def _pad(x, pw):
    flat = tuple(v for pair in reversed(pw) for v in pair)
    return torch.nn.functional.pad(x, flat)

kernel_name = 'sequential_preLN__sliding_local_dilated__mixer_token_mlp__prompt'

def setup_inputs():

    _w = (lambda a: (lambda t: t.to(torch.bfloat16) if t.is_floating_point() else t)(torch.as_tensor(a)))
    def _A_setup():
        B, S, D = 1, 2048, 512
        return {
            "x": _w((lambda *s: torch.randn(*s, dtype=torch.bfloat16) * 0.05)(B, S, D)),
            "wqkv": _w((lambda *s: torch.randn(*s, dtype=torch.bfloat16) * 0.05)(D, 3 * D)),
            "wo": _w((lambda *s: torch.randn(*s, dtype=torch.bfloat16) * 0.05)(D, D)),
        }
    _A_in = _A_setup()

    def _B_setup():
        B, S, D, F = 1, 2048, 512, 1792
        return {
            "x": _w((lambda *s: torch.randn(*s, dtype=torch.bfloat16) * 0.05)(B, S, D)),
            "w1": _w((lambda *s: torch.randn(*s, dtype=torch.bfloat16) * 1.6)(S, F) * (S ** -0.5)),
            "b1": _w((lambda *s: torch.randn(*s, dtype=torch.bfloat16) * 12.8)(F) * 0.02),
            "w2": _w((lambda *s: torch.randn(*s, dtype=torch.bfloat16) * 0.8)(F, S) * (F ** -0.5)),
            "b2": _w((lambda *s: torch.randn(*s, dtype=torch.bfloat16) * 12.8)(S) * 0.02),
        }
    _B_in = _B_setup()

    return {
        "A_x": _A_in["x"],
        "A_wqkv": _A_in["wqkv"],
        "A_wo": _A_in["wo"],
        "B_x": _B_in["x"],
        "B_w1": _B_in["w1"],
        "B_b1": _B_in["b1"],
        "B_w2": _B_in["w2"],
        "B_b2": _B_in["b2"],
    }

def reference(A_x, A_wqkv, A_wo, B_x, B_w1, B_b1, B_w2, B_b2):
    def _A_ref(x, wqkv, wo):
        B, S, D = x.shape
        H = 32; Dh = D // H
        W, dil = 8, 2
        qkv = (lambda x, *d: x.permute(*d))((x @ wqkv).reshape(B, S, 3, H, Dh), 2, 0, 3, 1, 4)
        q, k, v = qkv[0], qkv[1], qkv[2]
        scale = Dh ** -0.5
        pos = torch.arange(S)
        off = torch.arange(W) * dil
        idx = pos[:, None] - off[None, :]
        mask = idx >= 0
        idx_c = torch.where(mask, idx, 0).reshape(-1)
        kg = k[:, :, idx_c, :].reshape(B, H, S, W, Dh)
        vg = v[:, :, idx_c, :].reshape(B, H, S, W, Dh)
        logits = torch.einsum("bhsd,bhswd->bhsw", q, kg) * scale
        logits = torch.where(mask[None, None, :, :], logits, -1e9)
        attn = _softmax(logits, axis=-1)
        o = torch.einsum("bhsw,bhswd->bhsd", attn, vg)
        return (lambda x, *d: x.permute(*d))(o, 0, 2, 1, 3).reshape(B, S, D) @ wo

    def _B_ref(x, w1, b1, w2, b2):
        B, S, D = x.shape
        h = _layernorm(x, axis=-1)
        u = torch.einsum("bsd,sf->bdf", h, w1) + b1
        u = _gelu(u)
        y = torch.einsum("bdf,fs->bds", u, w2) + b2
        return (lambda x, *d: x.permute(*d))(y, 0, 2, 1)

    x = A_x
    h = x + _A_ref(_layernorm(x, axis=-1), A_wqkv, A_wo)
    h2 = h + _B_ref(_layernorm(h, axis=-1), B_w1, B_b1, B_w2, B_b2)
    return h2
```

```cpp
#include <hip/hip_runtime.h>
#include <hip/hip_bf16.h>
typedef unsigned short u16;
__device__ __forceinline__ float bf2f(u16 b){ return __builtin_bit_cast(float, (unsigned)b<<16); }
__device__ __forceinline__ u16 f2bf(float f){ __bf16 h=(__bf16)f; return __builtin_bit_cast(u16,h); }
constexpr int S=2048,D=512,F=1792,H=32,DH=16;
__global__ void k_ln(const u16* x, u16* y, int twice){
  int s=blockIdx.x, l=threadIdx.x; float v[8]; float sum=0;
  for(int j=0;j<8;j++){v[j]=bf2f(x[s*D+l*8+j]); sum+=v[j];}
  for(int it=0; it<=twice; ++it){
    sum=0; for(int j=0;j<8;j++) sum+=v[j];
    for(int o=32;o;o>>=1) sum+=__shfl_xor(sum,o);
    float m=sum/D; float q=0; for(int j=0;j<8;j++){float d=v[j]-m; q+=d*d;}
    for(int o=32;o;o>>=1) q+=__shfl_xor(q,o);
    float r=1.0f/sqrtf(q/D+1e-5f);
    for(int j=0;j<8;j++) v[j]=(v[j]-m)*r;
  }
  for(int j=0;j<8;j++) y[s*D+l*8+j]=f2bf(v[j]);
}
template<int MODE>
__global__ void k_gemm(const u16* A,int lam,int lak,const u16* B,int ldb,int K,int N,u16* C,const u16* bias,const u16* res){
  int n=blockIdx.x*64+(threadIdx.x&63); int m=blockIdx.y*4+(threadIdx.x>>6);
  float acc=0; for(int k=0;k<K;k++) acc+=bf2f(A[m*lam+k*lak])*bf2f(B[k*ldb+n]);
  if(MODE==0) C[m*N+n]=f2bf(acc);
  if(MODE==1) C[m*N+n]=f2bf(bf2f(f2bf(acc))+bf2f(res[m*N+n]));
  if(MODE==2){ float u=acc+bf2f(bias[n]); float t=0.7978845608f*(u+0.044715f*u*u*u); C[m*N+n]=f2bf(0.5f*u*(1.f+tanhf(t))); }
  if(MODE==3){ float y=acc+bf2f(bias[n]); C[n*D+m]=f2bf(y+bf2f(res[n*D+m])); }
}
__global__ void k_attn(const u16* qkv,u16* o){
  int s=blockIdx.x, h=threadIdx.x; if(h>=H) return;
  float q[DH]; for(int d=0;d<DH;d++) q[d]=bf2f(qkv[s*1536+h*16+d]);
  float lg[8]; float mx=-1e30f;
  for(int w=0;w<8;w++){ int t=s-2*w; float a; if(t>=0){a=0; for(int d=0;d<DH;d++) a+=q[d]*bf2f(qkv[t*1536+512+h*16+d]); a*=0.25f;} else a=-1e9f; lg[w]=a; mx=fmaxf(mx,a);}
  float sum=0; for(int w=0;w<8;w++){lg[w]=__expf(lg[w]-mx); sum+=lg[w];}
  float acc[DH]; for(int d=0;d<DH;d++) acc[d]=0;
  for(int w=0;w<8;w++){ int t=s-2*w; if(t<0) t=0; for(int d=0;d<DH;d++) acc[d]+=lg[w]/sum*bf2f(qkv[t*1536+1024+h*16+d]); }
  for(int d=0;d<DH;d++) o[s*D+h*16+d]=f2bf(acc[d]);
}
extern "C" void kernel_launch(void* const* d_in, const int* in_sizes, int n_in, void* d_out, int out_size, void* d_ws, size_t ws_size, hipStream_t stream) {
  const u16* x=(const u16*)d_in[0]; const u16* wqkv=(const u16*)d_in[1]; const u16* wo=(const u16*)d_in[2];
  const u16* w1=(const u16*)d_in[4]; const u16* b1=(const u16*)d_in[5]; const u16* w2=(const u16*)d_in[6]; const u16* b2=(const u16*)d_in[7];
  u16* ws=(u16*)d_ws; u16* xn=ws; u16* qkv=xn+S*D; u16* o=qkv+S*1536; u16* h=o+S*D; u16* hn=h+S*D; u16* u=hn+S*D;
  k_ln<<<S,64,0,stream>>>(x,xn,0);
  k_gemm<0><<<dim3(1536/64,S/4),256,0,stream>>>(xn,D,1,wqkv,1536,D,1536,qkv,nullptr,nullptr);
  k_attn<<<S,64,0,stream>>>(qkv,o);
  k_gemm<1><<<dim3(D/64,S/4),256,0,stream>>>(o,D,1,wo,D,D,D,h,nullptr,x);
  k_ln<<<S,64,0,stream>>>(h,hn,1);
  k_gemm<2><<<dim3(F/64,D/4),256,0,stream>>>(hn,1,D,w1,F,S,F,u,b1,nullptr);
  k_gemm<3><<<dim3(S/64,D/4),256,0,stream>>>(u,F,1,w2,S,F,S,(u16*)d_out,b2,h);
}
```

```cpp
#include <hip/hip_runtime.h>

typedef unsigned short u16;
typedef unsigned u32;
typedef unsigned long long u64;
typedef __bf16 bf16x8 __attribute__((ext_vector_type(8)));
typedef __bf16 bf16x2 __attribute__((ext_vector_type(2)));
typedef short s16x4 __attribute__((ext_vector_type(4)));
typedef float f32x4 __attribute__((ext_vector_type(4)));
typedef float f32x2 __attribute__((ext_vector_type(2)));
typedef u32 u32x4 __attribute__((ext_vector_type(4)));
typedef u32 u32x2 __attribute__((ext_vector_type(2)));
#define LAS __attribute__((address_space(3)))
#define GAS __attribute__((address_space(1)))
#define DI __device__ __forceinline__
#define RLX_AGENT __ATOMIC_RELAXED, __HIP_MEMORY_SCOPE_AGENT
typedef __amdgpu_buffer_rsrc_t rsrc_t;

constexpr int S = 2048, D = 512, F = 1792;
constexpr float EPS = 1e-5f;
constexpr u32 WS_EPOCH = 0;
constexpr u32 WS_F1 = 4096;
constexpr u32 WS_F2 = WS_F1 + 256 * 128;
constexpr u32 WS_F3 = WS_F2 + 256 * 256;
constexpr u32 WS_ST = WS_F3 + 256 * 256;
constexpr u32 WS_O = 1u << 20;
constexpr u32 WS_HN = WS_O + (2u << 20);
constexpr u32 WS_U = WS_HN + (2u << 20);
constexpr u32 WS_END = WS_U + (2u << 20);
constexpr u32 WS_STAMP = WS_END;
constexpr u32 L_X = 0;
constexpr u32 L_RING = 81920;
constexpr u32 L_H = 147456;
constexpr u32 L_MISC = 155648;
constexpr u32 L_PROW = L_MISC;
constexpr u32 L_PCS = L_MISC + 3072;
constexpr u32 L_VT = L_MISC + 4608;
constexpr u32 L_B1 = L_MISC + 7680;
constexpr u32 L_B2 = L_MISC + 7936;
constexpr u32 L_TOTAL = 163840;
constexpr u32 L_PS = L_MISC;
constexpr u32 L_TR = L_MISC + 2048;
constexpr int SPIN_CAP = 1 << 18;

DI void dma16(u32 lds, u32 voff, rsrc_t rs, u32 soff) {
  asm volatile("s_mov_b32 m0, %0\n\ts_nop 0\n\tbuffer_load_dwordx4 %1, %2, %3 offen lds" ::"s"(lds), "v"(voff), "s"(rs), "s"(soff) : "memory");
}
DI void dma4(u32 lds, u32 voff, rsrc_t rs, u32 soff) {
  asm volatile("s_mov_b32 m0, %0\n\ts_nop 0\n\tbuffer_load_dword %1, %2, %3 offen lds" ::"s"(lds), "v"(voff), "s"(rs), "s"(soff) : "memory");
}
#define WAIT_VM(n) asm volatile("s_waitcnt vmcnt(" #n ")" ::: "memory")
#define WAIT_LGKM0() asm volatile("s_waitcnt lgkmcnt(0)" ::: "memory")
#define BARRIER() do { asm volatile("s_waitcnt lgkmcnt(0)\n\ts_barrier" ::: "memory"); } while (0)
#define SCHED() __builtin_amdgcn_sched_barrier(0)

DI f32x4 mfma32(u32x4 a, u32x4 b, f32x4 c) {
  return __builtin_amdgcn_mfma_f32_16x16x32_bf16(__builtin_bit_cast(bf16x8, a), __builtin_bit_cast(bf16x8, b), c, 0, 0, 0);
}
DI f32x4 mfma16(u32x2 a, u32x2 b, f32x4 c) {
  return __builtin_amdgcn_mfma_f32_16x16x16bf16_1k(__builtin_bit_cast(s16x4, a), __builtin_bit_cast(s16x4, b), c, 0, 0, 0);
}
DI u32x2 trd(LAS char* L, u32 addr) {
  return __builtin_bit_cast(u32x2, __builtin_amdgcn_ds_read_tr16_b64_v4i16((LAS s16x4*)(L + addr)));
}
DI u32x4 trfrag(LAS char* L, u32 a0, u32 a1) {
  u32x2 lo = trd(L, a0), hi = trd(L, a1);
  return u32x4{lo.x, lo.y, hi.x, hi.y};
}
DI u32 pk(float a, float b) {
  f32x2 v = {a, b};
  return __builtin_bit_cast(u32, __builtin_convertvector(v, bf16x2));
}
DI float blo(u32 v) { return __builtin_bit_cast(float, v << 16); }
DI float bhi(u32 v) { return __builtin_bit_cast(float, v & 0xffff0000u); }
DI float dot2(u32 a, u32 b, float c) {
  return __builtin_amdgcn_fdot2_f32_bf16(__builtin_bit_cast(bf16x2, a), __builtin_bit_cast(bf16x2, b), c, false);
}
DI u32 f2u(float f) { return __builtin_bit_cast(u32, f); }
DI float u2f(u32 u) { return __builtin_bit_cast(float, u); }
DI float red4(float a, float b, float c, float d) {
  auto r1 = __builtin_amdgcn_permlane16_swap(f2u(a), f2u(b), false, false);
  auto r2 = __builtin_amdgcn_permlane16_swap(f2u(c), f2u(d), false, false);
  float ab = u2f(r1[0]) + u2f(r1[1]), cd = u2f(r2[0]) + u2f(r2[1]);
  auto r3 = __builtin_amdgcn_permlane32_swap(f2u(ab), f2u(cd), false, false);
  return u2f(r3[0]) + u2f(r3[1]);
}
DI float allsum4(float a) {
  auto r1 = __builtin_amdgcn_permlane16_swap(f2u(a), f2u(a), false, false);
  float s = u2f(r1[0]) + u2f(r1[1]);
  auto r2 = __builtin_amdgcn_permlane32_swap(f2u(s), f2u(s), false, false);
  return u2f(r2[0]) + u2f(r2[1]);
}
DI float allmax4(float a) {
  auto r1 = __builtin_amdgcn_permlane16_swap(f2u(a), f2u(a), false, false);
  float s = fmaxf(u2f(r1[0]), u2f(r1[1]));
  auto r2 = __builtin_amdgcn_permlane32_swap(f2u(s), f2u(s), false, false);
  return fmaxf(u2f(r2[0]), u2f(r2[1]));
}
DI u32x4 ld_sc1(rsrc_t rs, u32 voff, u32 soff) { return __builtin_amdgcn_raw_buffer_load_b128(rs, voff, soff, 16); }
DI void st_sc1(u32x4 v, rsrc_t rs, u32 voff, u32 soff) { __builtin_amdgcn_raw_buffer_store_b128(v, rs, voff, soff, 16); }

DI void poll_tag(const GAS u64* p, u64 tag) {
  for (int sp = 0; sp < SPIN_CAP; ++sp) {
    u64 f = __hip_atomic_load(p, RLX_AGENT);
    if (__all(f == tag)) break;
  }
  asm volatile("" ::: "memory");
}

#define TS(i)

__global__ __launch_bounds__(512, 2) void fused_block(const u16* __restrict__ x, const u16* __restrict__ wqkv, const u16* __restrict__ wo,
                                                      const u16* __restrict__ w1, const u16* __restrict__ b1, const u16* __restrict__ w2,
                                                      const u16* __restrict__ b2, u16* __restrict__ out, char* __restrict__ ws) {
  extern __shared__ __attribute__((aligned(16))) char smem[];
  LAS char* L = (LAS char*)smem;
  TS(0);
  const u32 tid = threadIdx.x, lane = tid & 63;
  const u32 w = __builtin_amdgcn_readfirstlane(tid >> 6);
  const u32 b = blockIdx.x, xcd = b & 7, idx = b >> 3;
  const u32 c = (xcd & 1) * 4 + (idx & 3), tl = idx >> 2, sq = xcd >> 1, t = sq * 8 + tl;
  const u32 g = t * 8 + c;
  const bool p3 = tl < 7;
  const u32 j = sq * 7 + (p3 ? tl : 0);
  const u32 r16 = lane & 15, q = lane >> 4;
  const u32 hh = w & 3, th = w >> 2;

  const rsrc_t rs_x = __builtin_amdgcn_make_buffer_rsrc((void*)x, 0, S * D * 2, 0x00020000);
  const rsrc_t rs_wqkv = __builtin_amdgcn_make_buffer_rsrc((void*)wqkv, 0, D * 3 * D * 2, 0x00020000);
  const rsrc_t rs_wo = __builtin_amdgcn_make_buffer_rsrc((void*)wo, 0, D * D * 2, 0x00020000);
  const rsrc_t rs_w1 = __builtin_amdgcn_make_buffer_rsrc((void*)w1, 0, S * F * 2, 0x00020000);
  const rsrc_t rs_w2 = __builtin_amdgcn_make_buffer_rsrc((void*)w2, 0, F * S * 2, 0x00020000);
  const rsrc_t rs_ws = __builtin_amdgcn_make_buffer_rsrc((void*)ws, 0, WS_END, 0x00020000);

  u64 epoch;
  asm volatile("s_load_dwordx2 %0, %1, 0x0" : "=s"(epoch) : "s"(ws));

  if (w == 0) {
    const rsrc_t rs_b1 = __builtin_amdgcn_make_buffer_rsrc((void*)b1, 0, F * 2, 0x00020000);
    const rsrc_t rs_b2 = __builtin_amdgcn_make_buffer_rsrc((void*)b2, 0, S * 2, 0x00020000);
    asm volatile("s_nop 4" ::: "memory");
    dma4(L_B1, lane * 4, rs_b1, j * 128);
    dma4(L_B2, lane * 4, rs_b2, t * 128);
    __builtin_amdgcn_raw_buffer_store_b64(u32x2{0xffffffffu, 0xffffffffu}, rs_ws, lane * 8, WS_ST + g * 512, 16);
  }
  if (w == 1) {
    u64 pc;
    u32 nbytes;
    asm volatile("s_getpc_b64 %0\n.Lfb_pc_mark:\n\ts_mov_b32 %1, .Lfb_end_mark-.Lfb_pc_mark" : "=s"(pc), "=s"(nbytes));
    const rsrc_t rs_pc = __builtin_amdgcn_make_buffer_rsrc((void*)(pc & ~(u64)15), 0, nbytes, 0x00020000);
    asm volatile("s_nop 4" ::: "memory");
    dma16(L_MISC, lane * 16, rs_pc, idx * 1024);
  }
  const u32 rot = tl;
  {
    const u32 hA = rot >> 2;
    u32 xv[5];
#pragma unroll
    for (int i = 0; i < 5; ++i) {
      const u32 row = (w * 5 + i) * 2 + (lane >> 5);
      int tok = (int)(t * 64 + row) - 16;
      tok = tok < 0 ? 0 : tok;
      const u32 pos = lane & 31;
      xv[i] = (u32)tok * 1024 + ((pos ^ (row & 15)) << 4);
    }
    const u32 fW = ((lane >> 4) & 1) | ((w & 1) << 1);
    const u32 wv = (w * 8 + (lane >> 3)) * 3072 + c * 128 + (((lane & 7) ^ (fW << 1)) << 4);
#pragma unroll
    for (int i = 0; i < 5; ++i) dma16(L_X + hA * 40960 + (w * 5 + i) * 1024, xv[i], rs_x, hA * 512);
#pragma unroll
    for (int s = 0; s < 3; ++s) dma16(L_RING + 1 * 24576 + s * 8192 + w * 1024, wv, rs_wqkv, ((rot + 0) & 7) * (64 * 3072) + s * 1024);
#pragma unroll
    for (int i = 0; i < 5; ++i) dma16(L_X + (hA ^ 1) * 40960 + (w * 5 + i) * 1024, xv[i], rs_x, (hA ^ 1) * 512);
#pragma unroll
    for (int s = 0; s < 3; ++s) dma16(L_RING + 2 * 24576 + s * 8192 + w * 1024, wv, rs_wqkv, ((rot + 1) & 7) * (64 * 3072) + s * 1024);
#pragma unroll
    for (int s = 0; s < 3; ++s) dma16(L_RING + 0 * 24576 + s * 8192 + w * 1024, wv, rs_wqkv, ((rot + 2) & 7) * (64 * 3072) + s * 1024);
  }
  asm volatile("s_waitcnt lgkmcnt(0)" : "+s"(epoch));
  const u64 tag = (epoch + 1) * 0x9E3779B97F4A7C15ull + 0x632BE59BD9B4E019ull;

  f32x4 aQ[2], aK[3], aV[3];
#pragma unroll
  for (int i = 0; i < 2; ++i) aQ[i] = f32x4{0, 0, 0, 0};
#pragma unroll
  for (int i = 0; i < 3; ++i) { aK[i] = f32x4{0, 0, 0, 0}; aV[i] = f32x4{0, 0, 0, 0}; }
  float sS[3] = {0, 0, 0}, sQ[3] = {0, 0, 0}, sC[3] = {0, 0, 0};
  const u32 fW = ((lane >> 4) & 1) | ((w & 1) << 1);
  const u32 wv = (w * 8 + (lane >> 3)) * 3072 + c * 128 + (((lane & 7) ^ (fW << 1)) << 4);
  const u32 wov = (w * 8 + (lane >> 3)) * 1024 + c * 128 + (((lane & 7) ^ (fW << 1)) << 4);
  const u32 qp = (lane & 15) >> 2, pp = lane & 3;
  const u32 fN = ((qp >> 1) & 1) | ((q & 1) << 1);
  const u32 trN = (8 * q + qp) * 128 + 8 * pp;
  const u32 xrow = (th * 32 + r16) * 512;
  u32 slot = 1;
#pragma unroll 1
  for (u32 i = 0; i < 8; ++i) {
    if (i == 0) WAIT_VM(11); else WAIT_VM(3);
    BARRIER();
    if (i == 0) TS(1);
    if (i >= 1) {
      const u32 fs = slot == 0 ? 2 : slot - 1;
      if (i <= 5) {
        const u32 so = ((rot + i + 2) & 7) * (64 * 3072);
#pragma unroll
        for (int s = 0; s < 3; ++s) dma16(L_RING + fs * 24576 + s * 8192 + w * 1024, wv, rs_wqkv, so + s * 1024);
      } else {
#pragma unroll
        for (int s = 0; s < 3; ++s) dma16(L_RING + fs * 24576 + s * 8192 + w * 1024, wov, rs_wo, (fs * 3 + s) * 65536);
      }
    }
    const u32 sg = (rot + i) & 7;
    const u32 xa0 = L_X + (sg >> 2) * 40960 + ((sg >> 1) & 1) * 256 + xrow + ((((sg & 1) * 8 + q) ^ r16) << 4);
    const u32 xa1 = xa0 ^ 64;
    const u32 wa = L_RING + slot * 24576 + trN + ((hh ^ fN) << 5);
    u32x4 xf[2][3], wf[2][3];
#pragma unroll
    for (int k = 0; k < 2; ++k) {
#pragma unroll
      for (int s = 0; s < 3; ++s) wf[k][s] = trfrag(L, wa + s * 8192 + k * 4096, wa + s * 8192 + k * 4096 + 512);
#pragma unroll
      for (int a = 0; a < 3; ++a) xf[k][a] = *(LAS u32x4*)(L + (k ? xa1 : xa0) + a * 8192);
    }
#pragma unroll
    for (int k = 0; k < 2; ++k) {
      aQ[0] = mfma32(wf[k][0], xf[k][1], aQ[0]);
      aQ[1] = mfma32(wf[k][0], xf[k][2], aQ[1]);
#pragma unroll
      for (int a = 0; a < 3; ++a) aK[a] = mfma32(wf[k][1], xf[k][a], aK[a]);
#pragma unroll
      for (int a = 0; a < 3; ++a) aV[a] = mfma32(xf[k][a], wf[k][2], aV[a]);
    }
    if (((i + 2 * th) & 3) == hh) {
#pragma unroll
      for (int k = 0; k < 2; ++k)
#pragma unroll
        for (int e = 0; e < 4; ++e)
#pragma unroll
          for (int a = 0; a < 3; ++a) {
            sS[a] = dot2(xf[k][a][e], 0x3f803f80u, sS[a]);
            sQ[a] = dot2(xf[k][a][e], xf[k][a][e], sQ[a]);
          }
    }
    if (((i + hh + 1) & 1) == th) {
#pragma unroll
      for (int k = 0; k < 2; ++k)
#pragma unroll
        for (int e = 0; e < 4; ++e)
#pragma unroll
          for (int s = 0; s < 3; ++s) sC[s] = dot2(wf[k][s][e], 0x3f803f80u, sC[s]);
    }
    slot = slot == 2 ? 0 : slot + 1;
  }
  TS(2);
  u32x2 xres[2];
  {
    const u32 col = c * 64 + hh * 16 + q * 4;
    const u32 half = col >> 8, ch = (col & 255) >> 3;
#pragma unroll
    for (int tt = 0; tt < 2; ++tt) {
      const u32 row = 16 + th * 32 + tt * 16 + r16;
      xres[tt] = *(LAS u32x2*)(L + L_X + half * 40960 + row * 512 + ((ch ^ r16) << 4) + (q & 1) * 8);
    }
  }
  {
    const float g1 = red4(sS[0], sQ[0], sS[1], sQ[1]);
    const float g2 = red4(sS[2], sQ[2], sC[0], sC[1]);
    const float g3 = allsum4(sC[2]);
    *(LAS float*)(L + L_PROW + ((th * 6 + q) * 16 + r16) * 16 + hh * 4) = g1;
    const u32 a2 = q < 2 ? L_PROW + ((th * 6 + 4 + q) * 16 + r16) * 16 + hh * 4 : L_PCS + ((hh * 3 + (q - 2)) * 16 + r16) * 8 + th * 4;
    *(LAS float*)(L + a2) = g2;
    *(LAS float*)(L + L_PCS + ((hh * 3 + 2) * 16 + r16) * 8 + th * 4) = g3;
  }
  BARRIER();
  const u32 w1v = (lane >> 3) * (F * 2) + (((lane & 7) ^ (((lane >> 4) & 3) << 1)) << 4);
  const u32 w1s = w * 256 * (F * 2) + j * 128;
#pragma unroll
  for (int s = 0; s < 2; ++s) dma16(L_RING + 2 * 24576 + s * 8192 + w * 1024, wov, rs_wo, (6 + s) * 65536);
  if (p3) {
#pragma unroll
    for (int p = 0; p < 8; ++p) dma16(w * 8192 + p * 1024, w1v, rs_w1, w1s + p * 8 * (F * 2));
  }
  {
    float mT[3], rT[3];
#pragma unroll
    for (int a = 0; a < 3; ++a) {
      const f32x4 ps = *(LAS f32x4*)(L + L_PROW + ((th * 6 + 2 * a) * 16 + r16) * 16);
      const f32x4 pq = *(LAS f32x4*)(L + L_PROW + ((th * 6 + 2 * a + 1) * 16 + r16) * 16);
      const float m = ((ps.x + ps.y) + (ps.z + ps.w)) * (1.0f / D);
      float v = ((pq.x + pq.y) + (pq.z + pq.w)) * (1.0f / D) - m * m;
      v = fmaxf(v, 0.0f);
      mT[a] = m;
      rT[a] = __builtin_amdgcn_rsqf(v + EPS);
      *(LAS f32x2*)(L + L_VT + ((w * 3 + a) * 16 + r16) * 8) = f32x2{mT[a], rT[a]};
    }
    asm volatile("" ::: "memory");
    float csQ[4], csK[4], csV;
    {
      const f32x4 a0 = *(LAS f32x4*)(L + L_PCS + ((hh * 3 + 0) * 16 + 4 * q) * 8);
      const f32x4 a1 = *(LAS f32x4*)(L + L_PCS + ((hh * 3 + 0) * 16 + 4 * q) * 8 + 16);
      const f32x4 k0 = *(LAS f32x4*)(L + L_PCS + ((hh * 3 + 1) * 16 + 4 * q) * 8);
      const f32x4 k1 = *(LAS f32x4*)(L + L_PCS + ((hh * 3 + 1) * 16 + 4 * q) * 8 + 16);
      const f32x2 v0 = *(LAS f32x2*)(L + L_PCS + ((hh * 3 + 2) * 16 + r16) * 8);
      csQ[0] = a0.x + a0.y; csQ[1] = a0.z + a0.w; csQ[2] = a1.x + a1.y; csQ[3] = a1.z + a1.w;
      csK[0] = k0.x + k0.y; csK[1] = k0.z + k0.w; csK[2] = k1.x + k1.y; csK[3] = k1.z + k1.w;
      csV = v0.x + v0.y;
    }
    u32x2 qf[2], kf[3], vf[3];
#pragma unroll
    for (int a = 0; a < 2; ++a) {
      float v[4];
#pragma unroll
      for (int e = 0; e < 4; ++e) v[e] = (aQ[a][e] - mT[a + 1] * csQ[e]) * rT[a + 1];
      qf[a] = u32x2{pk(v[0], v[1]), pk(v[2], v[3])};
    }
#pragma unroll
    for (int a = 0; a < 3; ++a) {
      float v[4];
#pragma unroll
      for (int e = 0; e < 4; ++e) v[e] = (aK[a][e] - mT[a] * csK[e]) * rT[a];
      kf[a] = u32x2{pk(v[0], v[1]), pk(v[2], v[3])};
    }
#pragma unroll
    for (int a = 0; a < 3; ++a) {
      const f32x4 s0 = *(LAS f32x4*)(L + L_VT + ((w * 3 + a) * 16 + 4 * q) * 8);
      const f32x4 s1 = *(LAS f32x4*)(L + L_VT + ((w * 3 + a) * 16 + 4 * q) * 8 + 16);
      vf[a] = u32x2{pk((aV[a][0] - s0.x * csV) * s0.y, (aV[a][1] - s0.z * csV) * s0.w),
                    pk((aV[a][2] - s1.x * csV) * s1.y, (aV[a][3] - s1.z * csV) * s1.w)};
    }
    u32x2 of[2];
    const bool odd = r16 & 1;
#pragma unroll
    for (int a = 0; a < 2; ++a) {
      const f32x4 z = {0, 0, 0, 0};
      const f32x4 sp = mfma16(kf[a], qf[a], z);
      const f32x4 so = mfma16(kf[a + 1], qf[a], z);
      float lg[2];
      bool own[2];
#pragma unroll
      for (int u = 0; u < 2; ++u) {
        own[u] = (4 * q + 2 * u + (r16 & 1)) <= r16;
        const float vo = odd ? so[2 * u + 1] : so[2 * u];
        float vp = odd ? sp[2 * u + 1] : sp[2 * u];
        if (a == 0) vp = (t == 0 && th == 0) ? -4e9f : vp;
        lg[u] = own[u] ? vo : vp;
      }
      const float mx = allmax4(fmaxf(lg[0], lg[1]));
      const float cs = 0.25f * 1.44269504088896f;
      const float e0 = __builtin_amdgcn_exp2f((lg[0] - mx) * cs), e1 = __builtin_amdgcn_exp2f((lg[1] - mx) * cs);
      const float inv = __builtin_amdgcn_rcpf(allsum4(e0 + e1));
      const u32 pw = pk(e0 * inv, e1 * inv);
      const u32 p0 = odd ? pw << 16 : pw & 0xffffu, p1 = odd ? pw & 0xffff0000u : pw >> 16;
      const u32x4 pf = {own[0] ? 0u : p0, own[1] ? 0u : p1, own[0] ? p0 : 0u, own[1] ? p1 : 0u};
      const u32x4 vv = {vf[a].x, vf[a].y, vf[a + 1].x, vf[a + 1].y};
      const f32x4 o = mfma32(vv, pf, z);
      of[a] = u32x2{pk(o[0], o[1]), pk(o[2], o[3])};
    }
    auto s0 = __builtin_amdgcn_permlane16_swap(of[0].x, of[1].x, false, false);
    auto s1 = __builtin_amdgcn_permlane16_swap(of[0].y, of[1].y, false, false);
    const u32x4 ov = {s0[0], s1[0], s0[1], s1[1]};
    const u32 ooff = (q & 1) * 2048 + ((q >> 1) * 16 + r16) * 16;
    st_sc1(ov, rs_ws, ooff, WS_O + g * 8192 + th * 4096 + (hh >> 1) * 1024 + (hh & 1) * 512);
  }
  TS(3);
  WAIT_VM(0);
  BARRIER();
  if (w == 0 && lane < 8) __hip_atomic_store((GAS u64*)(ws + WS_F1 + (t * 8 + lane) * 128 + c * 8), tag, RLX_AGENT);
  TS(4);

  u32x4 B1[8][4];
  const u32 fP = (2 * q + (qp >> 1)) & 3;
  const u32 trP = (4 * q + qp) * 128 + (fP << 5) + 8 * pp;
  u32x2 hreg[2];
  {
    u32x4 Bo[4][4];
    const u32 wa = L_RING + hh * 16384 + trN + ((hh ^ fN) << 5);
#pragma unroll
    for (int k = 0; k < 4; ++k)
#pragma unroll
      for (int n = 0; n < 4; ++n) Bo[k][n] = trfrag(L, (wa ^ (n << 5)) + k * 4096, (wa ^ (n << 5)) + k * 4096 + 512);
    if (p3) {
#pragma unroll
      for (int k = 0; k < 2; ++k)
#pragma unroll
        for (int n = 0; n < 4; ++n) B1[k][n] = trfrag(L, w * 8192 + (trP ^ (n << 5)) + k * 4096, w * 8192 + (trP ^ (n << 5)) + k * 4096 + 2048);
    }
    SCHED();
    WAIT_LGKM0();
    SCHED();
    if (p3) {
#pragma unroll
      for (int p = 0; p < 8; ++p) dma16(w * 8192 + p * 1024, w1v, rs_w1, w1s + (8 + p) * 8 * (F * 2));
    }
    BARRIER();
    poll_tag((const GAS u64*)(ws + WS_F1 + g * 128 + (2 * hh + (lane & 1)) * 8), tag);
    TS(5);
    u32x4 ofr[2][4];
#pragma unroll
    for (int k = 0; k < 4; ++k)
#pragma unroll
      for (int tt = 0; tt < 2; ++tt)
        ofr[tt][k] = ld_sc1(rs_ws, lane * 16 + (k >> 1) * 8192 + tt * 2048 + (k & 1) * 1024, WS_O + (t * 8 + 2 * hh) * 8192 + th * 4096);
    SCHED();
    f32x4 acc[2][4];
#pragma unroll
    for (int tt = 0; tt < 2; ++tt)
#pragma unroll
      for (int n = 0; n < 4; ++n) acc[tt][n] = f32x4{0, 0, 0, 0};
#pragma unroll
    for (int k = 0; k < 4; ++k)
#pragma unroll
      for (int tt = 0; tt < 2; ++tt)
#pragma unroll
        for (int n = 0; n < 4; ++n) acc[tt][n] = mfma32(Bo[k][n], ofr[tt][k], acc[tt][n]);
#pragma unroll
    for (int n = 1; n < 4; ++n)
#pragma unroll
      for (int tt = 0; tt < 2; ++tt)
        *(LAS f32x4*)(L + L_RING + (((th * 4 + (hh ^ n)) * 3 + (n - 1)) * 2 + tt) * 1024 + lane * 16) = acc[tt][n];
    BARRIER();
    TS(6);
    float hv[2][4];
    float st[4];
#pragma unroll
    for (int tt = 0; tt < 2; ++tt) {
      f32x4 a = acc[tt][0];
#pragma unroll
      for (int n = 1; n < 4; ++n) a += *(LAS f32x4*)(L + L_RING + ((w * 3 + (n - 1)) * 2 + tt) * 1024 + lane * 16);
      const u32 a01 = pk(a[0], a[1]), a23 = pk(a[2], a[3]);
      const u32 h01 = pk(blo(xres[tt].x) + blo(a01), bhi(xres[tt].x) + bhi(a01));
      const u32 h23 = pk(blo(xres[tt].y) + blo(a23), bhi(xres[tt].y) + bhi(a23));
      hreg[tt] = u32x2{h01, h23};
      hv[tt][0] = blo(h01); hv[tt][1] = bhi(h01); hv[tt][2] = blo(h23); hv[tt][3] = bhi(h23);
      st[2 * tt] = (hv[tt][0] + hv[tt][1]) + (hv[tt][2] + hv[tt][3]);
      st[2 * tt + 1] = (hv[tt][0] * hv[tt][0] + hv[tt][1] * hv[tt][1]) + (hv[tt][2] * hv[tt][2] + hv[tt][3] * hv[tt][3]);
    }
    const float pr = red4(st[0], st[1], st[2], st[3]);
    *(LAS float*)(L + L_PS + ((th * 32 + (q >> 1) * 16 + r16) * 2 + (q & 1)) * 16 + hh * 4) = pr;
#pragma unroll
    for (int tt = 0; tt < 2; ++tt) *(LAS u32x2*)(L + L_H + (th * 32 + tt * 16 + r16) * 128 + hh * 32 + q * 8) = hreg[tt];
    BARRIER();
    if (w == 0) {
      const f32x4 p0 = *(LAS f32x4*)(L + L_PS + lane * 32), p1 = *(LAS f32x4*)(L + L_PS + lane * 32 + 16);
      const u32 s1 = min(f2u((p0.x + p0.y) + (p0.z + p0.w)), 0xfffffffeu);
      const u32 s2 = min(f2u((p1.x + p1.y) + (p1.z + p1.w)), 0xfffffffeu);
      __builtin_amdgcn_raw_buffer_store_b64(u32x2{s1, s2}, rs_ws, lane * 8, WS_ST + g * 512, 16);
    }
    TS(7);
    if (p3) {
      WAIT_VM(0);
#pragma unroll
      for (int k = 0; k < 2; ++k)
#pragma unroll
        for (int n = 0; n < 4; ++n) B1[2 + k][n] = trfrag(L, w * 8192 + (trP ^ (n << 5)) + k * 4096, w * 8192 + (trP ^ (n << 5)) + k * 4096 + 2048);
      SCHED();
      WAIT_LGKM0();
      SCHED();
#pragma unroll
      for (int p = 0; p < 8; ++p) dma16(w * 8192 + p * 1024, w1v, rs_w1, w1s + (16 + p) * 8 * (F * 2));
    }
    u32x2 sv[2][2];
    for (int sp = 0; sp < SPIN_CAP; ++sp) {
      asm volatile("" ::: "memory");
#pragma unroll
      for (int tt = 0; tt < 2; ++tt)
#pragma unroll
        for (int e = 0; e < 2; ++e)
          sv[tt][e] = __builtin_amdgcn_raw_buffer_load_b64(rs_ws, (2 * q + e) * 512 + (tt * 16 + r16) * 8, WS_ST + t * 4096 + th * 256, 16);
      const u32 m1 = max(max(sv[0][0].x, sv[0][0].y), max(sv[0][1].x, sv[0][1].y));
      const u32 m2 = max(max(sv[1][0].x, sv[1][0].y), max(sv[1][1].x, sv[1][1].y));
      if (!__any(max(m1, m2) == 0xffffffffu)) break;
    }
    TS(8);
#pragma unroll
    for (int tt = 0; tt < 2; ++tt) {
      const float s1 = allsum4(u2f(sv[tt][0].x) + u2f(sv[tt][1].x));
      const float s2 = allsum4(u2f(sv[tt][0].y) + u2f(sv[tt][1].y));
      const float m = s1 * (1.0f / D);
      const float v = fmaxf(s2 * (1.0f / D) - m * m, 0.0f);
      const float r1 = __builtin_amdgcn_rsqf(v + EPS);
      const float r2 = __builtin_amdgcn_rsqf(v * r1 * r1 + EPS);
      const float r = r1 * r2;
      const u32x2 hn = {pk((hv[tt][0] - m) * r, (hv[tt][1] - m) * r), pk((hv[tt][2] - m) * r, (hv[tt][3] - m) * r)};
      *(LAS u32x2*)(L + L_RING + 49152 + w * 1024 + (tt * 16 + r16) * 32 + q * 8) = hn;
    }
    asm volatile("" ::: "memory");
    {
      const u32 ta = L_RING + 49152 + w * 1024 + (4 * q + qp) * 32 + pp * 8;
      const u32x4 hf = trfrag(L, ta, ta + 512);
      st_sc1(hf, rs_ws, lane * 16, WS_HN + c * 262144 + (2 * t + th) * 4096 + hh * 1024);
    }
    WAIT_VM(0);
    BARRIER();
    if (w == 0 && lane < 32) __hip_atomic_store((GAS u64*)(ws + WS_F2 + (lane * 8 + c) * 256 + t * 8), tag, RLX_AGENT);
    TS(9);
  }

  const u32 w2v = (lane >> 3) * (S * 2) + (((lane & 7) ^ (((lane >> 4) & 3) << 1)) << 4);
  const u32 w2s = w * 224 * (S * 2) + t * 128;
  if (p3) {
#pragma unroll
    for (int p = 0; p < 8; ++p) dma16(65536 + w * 8192 + p * 1024, w1v, rs_w1, w1s + (24 + p) * 8 * (F * 2));
#pragma unroll
    for (int k = 0; k < 2; ++k)
#pragma unroll
      for (int n = 0; n < 4; ++n) B1[4 + k][n] = trfrag(L, w * 8192 + (trP ^ (n << 5)) + k * 4096, w * 8192 + (trP ^ (n << 5)) + k * 4096 + 2048);
    WAIT_VM(0);
#pragma unroll
    for (int k = 0; k < 2; ++k)
#pragma unroll
      for (int n = 0; n < 4; ++n)
        B1[6 + k][n] = trfrag(L, 65536 + w * 8192 + (trP ^ (n << 5)) + k * 4096, 65536 + w * 8192 + (trP ^ (n << 5)) + k * 4096 + 2048);
    BARRIER();
    poll_tag((const GAS u64*)(ws + WS_F2 + g * 256 + (4 * w + (lane & 3)) * 8), tag);
    TS(10);
    constexpr int RING = 16;
    u32x4 A[RING];
    const u32 as = WS_HN + c * 262144 + w * 32768;
#pragma unroll
    for (int i = 0; i < RING; ++i) A[i] = ld_sc1(rs_ws, lane * 16 + (i >> 3) * 1024, as + (i & 7) * 4096);
    SCHED();
#pragma unroll
    for (int mt = 0; mt < 4; ++mt) {
      f32x4 acc[4];
#pragma unroll
      for (int n = 0; n < 4; ++n) acc[n] = f32x4{0, 0, 0, 0};
#pragma unroll
      for (int k = 0; k < 8; ++k) {
        const int i = mt * 8 + k;
        const u32x4 cur = A[i % RING];
#pragma unroll
        for (int n = 0; n < 4; ++n) acc[n] = mfma32(B1[k][n], cur, acc[n]);
        if (i + RING < 32) A[i % RING] = ld_sc1(rs_ws, lane * 16 + ((i + RING) >> 3) * 1024, as + ((i + RING) & 7) * 4096);
        SCHED();
      }
#pragma unroll
      for (int n = 0; n < 4; ++n) *(LAS f32x4*)(L + ((w * 4 + mt) * 4 + n) * 1024 + lane * 16) = acc[n];
    }
    BARRIER();
    TS(11);
    if (g == 0 && tid == 0) *(GAS u64*)(ws + WS_EPOCH) = epoch + 1;
    {
      const u32 mt = w >> 1, kp = w & 1;
      f32x4 s0 = {0, 0, 0, 0}, s1 = {0, 0, 0, 0};
#pragma unroll
      for (int ww = 0; ww < 8; ++ww) {
        s0 += *(LAS f32x4*)(L + ((ww * 4 + mt) * 4 + 2 * kp) * 1024 + lane * 16);
        s1 += *(LAS f32x4*)(L + ((ww * 4 + mt) * 4 + 2 * kp + 1) * 1024 + lane * 16);
      }
      const u32x2 ba = *(LAS u32x2*)(L + L_B1 + (kp * 32 + 4 * q) * 2), bb = *(LAS u32x2*)(L + L_B1 + (kp * 32 + 16 + 4 * q) * 2);
      float v[8] = {s0[0] + blo(ba.x), s0[1] + bhi(ba.x), s0[2] + blo(ba.y), s0[3] + bhi(ba.y),
                    s1[0] + blo(bb.x), s1[1] + bhi(bb.x), s1[2] + blo(bb.y), s1[3] + bhi(bb.y)};
#pragma unroll
      for (int e = 0; e < 8; ++e) {
        const float z = v[e] * (v[e] * v[e] * (-0.044715f * 2.302208198f) - 2.302208198f);
        v[e] = v[e] * __builtin_amdgcn_rcpf(1.0f + __builtin_amdgcn_exp2f(z));
      }
      const u32x4 uf = {pk(v[0], v[1]), pk(v[2], v[3]), pk(v[4], v[5]), pk(v[6], v[7])};
      st_sc1(uf, rs_ws, lane * 16, WS_U + c * (56 * 4096) + (2 * j + kp) * 4096 + mt * 1024);
    }
    WAIT_VM(0);
    BARRIER();
    if (w == 0 && lane < 32) __hip_atomic_store((GAS u64*)(ws + WS_F3 + (lane * 8 + c) * 256 + j * 8), tag, RLX_AGENT);
  }
  TS(12);

  {
    u32x4 B2[7][4];
#pragma unroll
    for (int p = 0; p < 16; ++p) dma16(w * 16384 + p * 1024, w2v, rs_w2, w2s + p * 8 * (S * 2));
#pragma unroll
    for (int k = 0; k < 4; ++k) {
      if (k == 0) WAIT_VM(12);
      if (k == 1) WAIT_VM(12);
      if (k == 2) WAIT_VM(12);
      if (k == 3) WAIT_VM(12);
#pragma unroll
      for (int n = 0; n < 4; ++n) B2[k][n] = trfrag(L, w * 16384 + (trP ^ (n << 5)) + k * 4096, w * 16384 + (trP ^ (n << 5)) + k * 4096 + 2048);
      if (k < 3) {
        SCHED();
        WAIT_LGKM0();
        SCHED();
#pragma unroll
        for (int p = 0; p < 4; ++p) dma16(w * 16384 + k * 4096 + p * 1024, w2v, rs_w2, w2s + (16 + 4 * k + p) * 8 * (S * 2));
      }
    }
#pragma unroll
    for (int k = 0; k < 3; ++k) {
      if (k == 0) WAIT_VM(8);
      if (k == 1) WAIT_VM(4);
      if (k == 2) WAIT_VM(0);
#pragma unroll
      for (int n = 0; n < 4; ++n) B2[4 + k][n] = trfrag(L, w * 16384 + (trP ^ (n << 5)) + k * 4096, w * 16384 + (trP ^ (n << 5)) + k * 4096 + 2048);
    }
    BARRIER();
    TS(13);
    poll_tag((const GAS u64*)(ws + WS_F3 + g * 256 + (((7 * w) >> 1) + (lane & 3)) * 8), tag);
    TS(14);
    constexpr int RING = 21;
    u32x4 A[RING];
    const u32 as = WS_U + c * (56 * 4096) + w * (7 * 4096);
#pragma unroll
    for (int i = 0; i < RING; ++i) A[i] = ld_sc1(rs_ws, lane * 16 + (i / 7) * 1024, as + (i % 7) * 4096);
    SCHED();
#pragma unroll
    for (int mt = 0; mt < 4; ++mt) {
      f32x4 acc[4];
#pragma unroll
      for (int n = 0; n < 4; ++n) acc[n] = f32x4{0, 0, 0, 0};
#pragma unroll
      for (int k = 0; k < 7; ++k) {
        const int i = mt * 7 + k;
        const u32x4 cur = A[i % RING];
#pragma unroll
        for (int n = 0; n < 4; ++n) acc[n] = mfma32(cur, B2[k][n], acc[n]);
        if (i + RING < 28) A[i % RING] = ld_sc1(rs_ws, lane * 16 + ((i + RING) / 7) * 1024, as + ((i + RING) % 7) * 4096);
        SCHED();
      }
      const u32 wl = (q * 16 + (r16 ^ ((2 * mt + (q >> 1)) << 1))) * 16;
#pragma unroll
      for (int n = 0; n < 4; ++n) *(LAS f32x4*)(L + ((w * 4 + mt) * 4 + n) * 1024 + wl) = acc[n];
    }
    BARRIER();
    {
      const u32 s = tid >> 3, gq = tid & 7;
      const u32 mt = gq >> 1, nt = s >> 4, s16 = s & 15;
      const u32 ra = ((mt * 4 + nt) * 1024) + ((gq & 1) * 32 + (s16 ^ (gq << 1))) * 16;
      f32x4 s0 = {0, 0, 0, 0}, s1 = {0, 0, 0, 0};
#pragma unroll
      for (int ww = 0; ww < 8; ++ww) {
        s0 += *(LAS f32x4*)(L + ww * 16384 + ra);
        s1 += *(LAS f32x4*)(L + ww * 16384 + ra + 256);
      }
      const u32x4 hq = *(LAS u32x4*)(L + L_H + s * 128 + gq * 16);
      const float bias = blo((u32) * (LAS u16*)(L + L_B2 + s * 2));
      const u32x4 ov = {pk(blo(hq.x) + (s0[0] + bias), bhi(hq.x) + (s0[1] + bias)), pk(blo(hq.y) + (s0[2] + bias), bhi(hq.y) + (s0[3] + bias)),
                        pk(blo(hq.z) + (s1[0] + bias), bhi(hq.z) + (s1[1] + bias)), pk(blo(hq.w) + (s1[2] + bias), bhi(hq.w) + (s1[3] + bias))};
      *(GAS u32x4*)(out + (size_t)(t * 64 + s) * D + c * 64 + gq * 8) = ov;
    }
  }
  asm volatile(".Lfb_end_mark:" ::: "memory");
}

extern "C" void kernel_launch(void* const* d_in, const int* in_sizes, int n_in, void* d_out, int out_size, void* d_ws, size_t ws_size,
                              hipStream_t stream) {
  (void)in_sizes; (void)n_in; (void)out_size; (void)ws_size;
  hipFuncSetAttribute((const void*)fused_block, hipFuncAttributeMaxDynamicSharedMemorySize, L_TOTAL);
  fused_block<<<256, 512, L_TOTAL, stream>>>((const u16*)d_in[0], (const u16*)d_in[1], (const u16*)d_in[2], (const u16*)d_in[4],
                                             (const u16*)d_in[5], (const u16*)d_in[6], (const u16*)d_in[7], (u16*)d_out, (char*)d_ws);
}
```

```cpp
#include <hip/hip_runtime.h>
#include <utility>
#pragma clang diagnostic ignored "-Wint-to-pointer-cast"

typedef unsigned short u16;
typedef unsigned u32;
typedef unsigned long long u64;
typedef __bf16 bf16x8 __attribute__((ext_vector_type(8)));
typedef __bf16 bf16x2 __attribute__((ext_vector_type(2)));
typedef short s16x4 __attribute__((ext_vector_type(4)));
typedef float f32x4 __attribute__((ext_vector_type(4)));
typedef float f32x2 __attribute__((ext_vector_type(2)));
typedef u32 u32x4 __attribute__((ext_vector_type(4)));
typedef u32 u32x2 __attribute__((ext_vector_type(2)));
#define LAS __attribute__((address_space(3)))
#define GAS __attribute__((address_space(1)))
#define DI __device__ __forceinline__
#define RLX_AGENT __ATOMIC_RELAXED, __HIP_MEMORY_SCOPE_AGENT
typedef __amdgpu_buffer_rsrc_t rsrc_t;

#define ABL_NOSTAT 0
#define ABL_NODMA 0
#define ABL_NOLDS 0
#define STAMP_TID 0
#define RING3 6
#define RING4 6
#define NT ""

constexpr int S = 2048, D = 512, F = 1792;
constexpr float EPS = 1e-5f;
constexpr u32 WS_EPOCH = 0;
constexpr u32 WS_F1 = 4096;
constexpr u32 WS_F2 = WS_F1 + 256 * 128;
constexpr u32 WS_F3 = WS_F2 + 256 * 256;
constexpr u32 WS_ST = WS_F3 + 256 * 256;
constexpr u32 WS_O = 1u << 20;
constexpr u32 WS_HN = WS_O + (2u << 20);
constexpr u32 WS_U = WS_HN + (2u << 20);
constexpr u32 WS_END = WS_U + (2u << 20);
constexpr u32 WS_STAMP = WS_END;
constexpr u32 L_RING = 0;
constexpr u32 L_X = 73728;
constexpr u32 L_MISC = 155648;
constexpr u32 L_PART = L_MISC;
constexpr u32 L_MR = L_MISC + 5120;
constexpr u32 L_ONES = L_MISC + 5888;
constexpr u32 L_CS = L_MISC + 4096;
constexpr u32 L_SCR = L_MISC + 6144;
constexpr u32 L_B1 = L_MISC + 7680;
constexpr u32 L_B2 = L_MISC + 7936;
constexpr u32 L_TOTAL = 163840;
constexpr int SPIN_CAP = 1 << 18;

DI void dma16(u32 lds, u32 voff, rsrc_t rs, u32 soff) {
  asm volatile("s_mov_b32 m0, %0\n\ts_nop 0\n\tbuffer_load_dwordx4 %1, %2, %3 offen" NT " lds" ::"s"(lds), "v"(voff), "s"(rs), "s"(soff) : "memory");
}
DI void dma4(u32 lds, u32 voff, rsrc_t rs, u32 soff) {
  asm volatile("s_mov_b32 m0, %0\n\ts_nop 0\n\tbuffer_load_dword %1, %2, %3 offen" NT " lds" ::"s"(lds), "v"(voff), "s"(rs), "s"(soff) : "memory");
}
template <int LC, int SC>
DI void dma16c(u32 lds, u32 voff, rsrc_t rs, u32 soff) {
  u32 tmp;
  asm volatile("s_add_u32 m0, %1, %5\n\ts_add_u32 %0, %4, %6\n\tbuffer_load_dwordx4 %2, %3, %0 offen" NT " lds"
               : "=&s"(tmp) : "s"(lds), "v"(voff), "s"(rs), "s"(soff), "n"(LC), "n"(SC) : "memory", "scc");
}
template <int STEP>
DI void dma16x4(u32 lds, u32 voffE, u32 voffO, rsrc_t rs, u32 soff) {
  u32 tmp;
  asm volatile(
      "s_mov_b32 m0, %1\n\t"
      "s_add_u32 %0, %5, %6\n\t"
      "buffer_load_dwordx4 %2, %4, %5 offen" NT " lds\n\t"
      "buffer_load_dwordx4 %3, %4, %5 offen offset:1024" NT " lds\n\t"
      "buffer_load_dwordx4 %2, %4, %0 offen offset:2048" NT " lds\n\t"
      "buffer_load_dwordx4 %3, %4, %0 offen offset:3072" NT " lds"
      : "=&s"(tmp)
      : "s"(lds), "v"(voffE), "v"(voffO), "s"(rs), "s"(soff), "n"(2 * STEP - 2048)
      : "memory", "scc");
}
template <int N> DI void wait_vm() { asm volatile("s_waitcnt vmcnt(%0)" ::"n"(N) : "memory"); }
#define WAIT_LGKM0() asm volatile("s_waitcnt lgkmcnt(0)" ::: "memory")
#define BARRIER() asm volatile("s_waitcnt lgkmcnt(0)\n\ts_barrier" ::: "memory")
#define SCHED() __builtin_amdgcn_sched_barrier(0)
#define FENCE() asm volatile("" ::: "memory")

DI f32x4 mfma32(u32x4 a, u32x4 b, f32x4 c) {
  return __builtin_amdgcn_mfma_f32_16x16x32_bf16(__builtin_bit_cast(bf16x8, a), __builtin_bit_cast(bf16x8, b), c, 0, 0, 0);
}
DI f32x4 mfma16(u32x2 a, u32x2 b, f32x4 c) {
  return __builtin_amdgcn_mfma_f32_16x16x16bf16_1k(__builtin_bit_cast(s16x4, a), __builtin_bit_cast(s16x4, b), c, 0, 0, 0);
}
DI u32x2 trd(u32 L, u32 addr) {
  return __builtin_bit_cast(u32x2, __builtin_amdgcn_ds_read_tr16_b64_v4i16((LAS s16x4*)(L + addr)));
}
DI u32x4 trfrag(u32 L, u32 a) {
  u32x2 lo = trd(L, a), hi = trd(L, a + 512);
  return u32x4{lo.x, lo.y, hi.x, hi.y};
}
DI u32 pk(float a, float b) {
  f32x2 v = {a, b};
  return __builtin_bit_cast(u32, __builtin_convertvector(v, bf16x2));
}
DI float blo(u32 v) { return __builtin_bit_cast(float, v << 16); }
DI float bhi(u32 v) { return __builtin_bit_cast(float, v & 0xffff0000u); }
DI u32 f2u(float f) { return __builtin_bit_cast(u32, f); }
DI float u2f(u32 u) { return __builtin_bit_cast(float, u); }
DI float vmax(float a, float b) { float r; asm("v_max_f32 %0, %1, %2" : "=v"(r) : "v"(a), "v"(b)); return r; }
DI float vadd(float a, float b) { float r; asm("v_add_f32 %0, %1, %2" : "=v"(r) : "v"(a), "v"(b)); return r; }
DI float red4(float a, float b, float c, float d) {
  auto r1 = __builtin_amdgcn_permlane16_swap(f2u(a), f2u(b), false, false);
  auto r2 = __builtin_amdgcn_permlane16_swap(f2u(c), f2u(d), false, false);
  float ab = vadd(u2f(r1[0]), u2f(r1[1])), cd = vadd(u2f(r2[0]), u2f(r2[1]));
  auto r3 = __builtin_amdgcn_permlane32_swap(f2u(ab), f2u(cd), false, false);
  return vadd(u2f(r3[0]), u2f(r3[1]));
}
template <bool MAX>
DI void allred2(float& a, float& b) {
  auto r1 = __builtin_amdgcn_permlane16_swap(f2u(a), f2u(b), false, false);
  float t = MAX ? vmax(u2f(r1[0]), u2f(r1[1])) : vadd(u2f(r1[0]), u2f(r1[1]));
  auto r2 = __builtin_amdgcn_permlane32_swap(f2u(t), f2u(t), false, false);
  float u = MAX ? vmax(u2f(r2[0]), u2f(r2[1])) : vadd(u2f(r2[0]), u2f(r2[1]));
  auto r3 = __builtin_amdgcn_permlane16_swap(f2u(u), f2u(u), false, false);
  a = u2f(r3[0]);
  b = u2f(r3[1]);
}
template <int CTRL> DI float dppadd(float v) { return v + __builtin_amdgcn_update_dpp(0.0f, v, CTRL, 0xf, 0xf, true); }
DI float sum8(float v) {
  v = dppadd<0xB1>(v);
  v = dppadd<0x4E>(v);
  v = dppadd<0x141>(v);
  return v;
}
template <int HI> DI void stat3(float& tmp, float& s, float& q, u32 v) {
  if constexpr (HI)
    asm volatile("v_and_b32 %0, 0xffff0000, %3\n\tv_add_f32 %1, %1, %0\n\tv_fmac_f32 %2, %0, %0" : "+v"(tmp), "+v"(s), "+v"(q) : "v"(v));
  else
    asm volatile("v_lshlrev_b32 %0, 16, %3\n\tv_add_f32 %1, %1, %0\n\tv_fmac_f32 %2, %0, %0" : "+v"(tmp), "+v"(s), "+v"(q) : "v"(v));
}
DI u32x4 ld_sc1(rsrc_t rs, u32 voff, u32 soff) { return __builtin_amdgcn_raw_buffer_load_b128(rs, voff, soff, 16); }
DI void st_sc1(u32x4 v, rsrc_t rs, u32 voff, u32 soff) { __builtin_amdgcn_raw_buffer_store_b128(v, rs, voff, soff, 16); }

DI void poll_tag(rsrc_t rs, u32 voff, u32 soff, u64 tag) {
  for (int sp = 0; sp < SPIN_CAP; ++sp) {
    FENCE();
    const u32x2 f = __builtin_amdgcn_raw_buffer_load_b64(rs, voff, soff, 16);
    if (__all(__builtin_bit_cast(u64, f) == tag)) break;
  }
  FENCE();
}
template <typename Fn, int... I> DI void sfor_impl(Fn&& f, std::integer_sequence<int, I...>) { (f(std::integral_constant<int, I>{}), ...); }
template <int N, typename Fn> DI void sfor(Fn&& f) { sfor_impl(f, std::make_integer_sequence<int, N>{}); }

#define TS(i)
#define TD(i)

__global__ __launch_bounds__(512, 2) void fused_block(const u16* __restrict__ x, const u16* __restrict__ wqkv, char* __restrict__ ws,
                                                      const u16* __restrict__ wo, const u16* __restrict__ w1, const u16* __restrict__ w2,
                                                      const u16* __restrict__ b1, const u16* __restrict__ b2, u16* __restrict__ out) {
  constexpr u32 L = 0;
  TS(0);
  const u32 tid = threadIdx.x, lane = tid & 63;
  const u32 w = __builtin_amdgcn_readfirstlane(tid >> 6);
  const u32 b = blockIdx.x, xcd = b & 7, idx = b >> 3;
  if (w == 7) {
    u64 pc;
    u32 nbytes;
    asm volatile("s_getpc_b64 %0\n.Lfb_pc_mark:\n\ts_mov_b32 %1, .Lfb_end_mark-.Lfb_pc_mark" : "=s"(pc), "=s"(nbytes));
    const rsrc_t rs_pc = __builtin_amdgcn_make_buffer_rsrc((void*)(pc & ~(u64)15), 0, nbytes, 0x00020000);
    asm volatile("s_mov_b32 m0, %0\n\ts_nop 0\n\tbuffer_load_dwordx4 %1, %2, %3 offen lds" ::"s"(L_SCR), "v"(lane * 16), "s"(rs_pc), "s"(idx * 1024) : "memory");
  }
  const u32 c = (xcd & 1) * 4 + (idx & 3), tl = idx >> 2, sq = xcd >> 1, t = sq * 8 + tl;
  const u32 g = t * 8 + c;
  const bool p3 = tl < 7;
  const u32 j = sq * 7 + (p3 ? tl : 0);
  const u32 r16 = lane & 15, q = lane >> 4;
  const u32 hh = w & 3, th = w >> 2;

  const rsrc_t rs_x = __builtin_amdgcn_make_buffer_rsrc((void*)x, 0, S * D * 2, 0x00020000);
  const rsrc_t rs_wqkv = __builtin_amdgcn_make_buffer_rsrc((void*)wqkv, 0, D * 3 * D * 2, 0x00020000);

  const u32 hA = ((tl >> 2) ^ c) & 1;
  const u32 xrw = 2 * w + (lane >> 5);
  const u32 xchunk = ((lane & 31) ^ xrw) << 4;
  const u32 xv = xrw * 1024 + xchunk;
  const u32 xv0 = t == 0 ? xchunk : xv;
  const u32 xs0 = t == 0 ? 0 : t * 65536 - 16384;
  const u32 swzE = ((lane & 7) ^ (((lane >> 4) & 1) << 1)) << 4;
  const u32 swzW = swzE ^ ((w & 1) << 6);
  const u32 wv = (w * 8 + (lane >> 3)) * 3072 + swzW;
  const u32 wdst = (w >> 2) * 12288 + (w & 3) * 1024;
  auto stage_of = [&](u32 s) { return ((hA ^ (s >> 2)) << 2) | ((s + tl) & 3); };
  const u32 xlA = w * 2048 + hA * 1024, xsA = t * 65536 + hA * 512;
  auto dma_x = [&](auto A_, auto H_) {
    constexpr int a = A_.value, h = H_.value;
    if constexpr (a == 0) dma16c<L_X, 0>(xlA ^ (h * 1024), xv0, rs_x, xs0 + ((hA ^ h) * 512));
    else dma16c<L_X + a * 16384, (a - 1) * 16384>(xlA ^ (h * 1024), xv, rs_x, xsA ^ (h * 512));
  };
  auto dma_w = [&](auto S_, auto G_) {
    constexpr int s = S_.value, seg = G_.value;
    dma16c<L_RING + ((s + 1) % 3) * 24576 + seg * 4096, seg * 1024>(wdst, wv, rs_wqkv, stage_of(s) * (64 * 3072) + c * 128);
  };
#define IC(n) std::integral_constant<int, (n)>{}
  asm volatile("s_nop 4" ::: "memory");
  sfor<5>([&](auto a) { dma_x(a, IC(0)); });
  sfor<3>([&](auto s) { dma_w(IC(0), s); });
  sfor<3>([&](auto s) { dma_w(IC(1), s); });

  const rsrc_t rs_wo = __builtin_amdgcn_make_buffer_rsrc((void*)wo, 0, D * D * 2, 0x00020000);
  const rsrc_t rs_w1 = __builtin_amdgcn_make_buffer_rsrc((void*)w1, 0, S * F * 2, 0x00020000);
  const rsrc_t rs_w2 = __builtin_amdgcn_make_buffer_rsrc((void*)w2, 0, F * S * 2, 0x00020000);
  const rsrc_t rs_ws = __builtin_amdgcn_make_buffer_rsrc((void*)ws, 0, WS_END, 0x00020000);
  u64 epoch;
  asm volatile("s_load_dwordx2 %0, %1, 0x0\n\ts_waitcnt lgkmcnt(0)" : "=s"(epoch) : "s"(ws));
  const u64 tag = (epoch + 1) * 0x9E3779B97F4A7C15ull + 0x632BE59BD9B4E019ull;
  *(LAS u32*)(L + L_ONES + (lane & 3) * 4) = 0x3f803f80u;

  const bool heavy = w >= 4;
  const u32 ta = heavy ? 0u : 1u;
  f32x4 aQ[2], aK[3], aV[3], aC;
  const f32x4 z4 = {0, 0, 0, 0};
  aC = z4;
#pragma unroll
  for (int i = 0; i < 2; ++i) aQ[i] = z4;
#pragma unroll
  for (int i = 0; i < 3; ++i) { aK[i] = z4; aV[i] = z4; }
  const u32 qp = (lane & 15) >> 2, pp = lane & 3;
  const u32 fN = ((qp >> 1) & 1) | ((q & 1) << 1);
  const u32 trN = (8 * q + qp) * 128 + (fN << 5) + 8 * pp;
  const u32 wr = trN ^ (hh << 5);
  const u32 xl = L_X + (r16 >> 1) * 2048 + (r16 & 1) * 512 + ((q ^ r16) << 4);
  u32x4 xres;
  const u32 w1E = (lane >> 3) * (F * 2) + swzE, w1O = (lane >> 3) * (F * 2) + (swzE ^ 64) + 8 * (F * 2) - 1024;
  const u32 rot1 = 2 * (c & 3);
  const u32 w1s = w * 256 * (F * 2) + j * 128;
  auto w1_issue = [&](int k, u32 lds) { dma16x4<8 * F * 2>(lds, w1E, w1O, rs_w1, w1s + ((k + rot1) & 7) * 32 * (F * 2)); };
  const u32 stgA = L_X + w * 4096, stgB = L_X + 32768 + w * 4096;

  auto p1_loop = [&](auto H_) {
    constexpr bool H = H_.value;
    u32x4 Xf[2][3], Wf[2][3], Sf[2];
    f32x4 sS[5], sQ[5];
    if constexpr (H) {
#pragma unroll
      for (int i = 0; i < 5; ++i) { sS[i] = z4; sQ[i] = z4; }
    }
    u32x4 ones = {0x3f803f80u, 0x3f803f80u, 0x3f803f80u, 0x3f803f80u};
    asm volatile("" : "+v"(ones));
    const u32 xb1 = xl + (H ? 1 : 3) * 16384;
    const u32 xb0 = r16 == 0 ? L_ONES : xl;
    const u32 xm0 = r16 == 0 ? 0u : ~0u;
    const u32 xst = xl ^ (hh << 6);
    const u32 xv2 = (xv ^ 128) + 8192, xv02 = t == 0 ? (xchunk ^ 128) : xv2;
    const u32 wov = (w * 8 + (lane >> 3)) * 1024 + swzW;
    u32 xa1, xa0, wr2 = wr + 32768;
    asm volatile("" : "+v"(wr2));
    u32x2 wh;
    auto rd = [&](auto HS_, auto N_) {
      constexpr int s = HS_.value / 2, kk = HS_.value & 1, n = N_.value;
      constexpr u32 so = L_RING + ((s + 1) % 3) * 24576 + kk * 12288;
      if constexpr (n == 0) {
        const u32 sg = stage_of(s);
        const u32 sb = ((sg >> 2) << 10) | ((sg & 3) << 7) | (kk << 6);
        xa1 = xb1 ^ sb;
        if constexpr (H) xa0 = __builtin_amdgcn_bitop3_b32(xb0, sb, xm0, 0x78);
        Xf[kk][1] = *(LAS u32x4*)(L + xa1);
      }
      if constexpr (n >= 1 && n <= 6) {
        constexpr int seg = (n - 1) / 2;
        constexpr u32 o = so + seg * 4096 + ((n - 1) & 1) * 512;
        const u32x2 v = o < 65536 ? trd(L, wr + o) : trd(L, wr2 + (o - 32768));
        if constexpr (n & 1) wh = v;
        else Wf[kk][seg] = u32x4{wh.x, wh.y, v.x, v.y};
      }
      if constexpr (n == 7) Xf[kk][2] = *(LAS u32x4*)(L + xa1 + 16384);
      if constexpr (n == 8) Xf[kk][0] = *(LAS u32x4*)(L + xa0);
    };
    auto mm = [&](auto KK_, auto N_) {
      constexpr int kk = KK_.value, n = N_.value;
      if constexpr (n == 0) aQ[0] = mfma32(Wf[kk][0], Xf[kk][1], aQ[0]);
      if constexpr (n == 1) aK[1] = mfma32(Wf[kk][1], Xf[kk][1], aK[1]);
      if constexpr (n == 2) aV[1] = mfma32(Xf[kk][1], Wf[kk][2], aV[1]);
      if constexpr (n == 3) aQ[1] = mfma32(Wf[kk][0], Xf[kk][2], aQ[1]);
      if constexpr (n == 4) aK[2] = mfma32(Wf[kk][1], Xf[kk][2], aK[2]);
      if constexpr (n == 5) aV[2] = mfma32(Xf[kk][2], Wf[kk][2], aV[2]);
      if constexpr (n == 6) aK[0] = mfma32(Wf[kk][1], Xf[kk][0], aK[0]);
      if constexpr (n == 7) aV[0] = mfma32(Xf[kk][0], Wf[kk][2], aV[0]);
      if constexpr (n == 8) aC = mfma32(Wf[kk][0], Xf[kk][0], aC);
    };
    auto stat_read = [&](auto F_) {
      constexpr int f = F_.value;
      Sf[f & 1] = *(LAS u32x4*)(L + ((xst ^ (((f / 10) ^ hA) << 10) ^ (((f / 5) & 1) << 8)) + (f % 5) * 16384));
    };
    auto stat_mm = [&](auto F_) {
      constexpr int f = F_.value;
      sS[f % 5] = mfma32(ones, Sf[f & 1], sS[f % 5]);
      sQ[f % 5] = mfma32(Sf[f & 1], Sf[f & 1], sQ[f % 5]);
    };
    const u32 spa = q == qp ? L_PART + r16 * 32 + hh * 8 : L_SCR - 2048 + 2048;
    const u32 sps = q == qp ? 512u : 0u;
    auto stat_out = [&](auto A_) {
      constexpr int a = A_.value;
      const bool b0 = r16 & 1, b1 = r16 & 2;
      const float d = b1 ? (b0 ? sQ[a].w : sQ[a].z) : (b0 ? sQ[a].y : sQ[a].x);
      *(LAS f32x2*)(L + spa + a * sps) = f32x2{sS[a].x, d};
    };
    auto stream = [&](auto B_, auto I_) {
      constexpr int B = B_.value, i = I_.value;
      if constexpr (B <= 5 && i < 6) {
        constexpr int s = B + 2, seg = i % 3, hi = i / 3;
        dma16c<L_RING + ((s + 1) % 3) * 24576 + hi * 12288 + seg * 4096, seg * 1024 + hi * 32 * 3072>(wdst, wv, rs_wqkv, stage_of(s) * (64 * 3072) + c * 128);
      } else if constexpr ((B == 1 || B == 2) && i < 11) {
        constexpr int a = i - 6, hi = B - 1;
        if constexpr (a == 0) dma16c<L_X + hi * 8192, 0>(xlA ^ 1024, hi ? xv02 : xv0, rs_x, xs0 + ((hA ^ 1) * 512));
        else dma16c<L_X + hi * 8192 + a * 16384, (a - 1) * 16384>(xlA ^ 1024, hi ? xv2 : xv, rs_x, xsA ^ 512);
      } else if constexpr (B >= 6 && i < (B == 8 ? 4 : 6)) {
        constexpr int p = (B - 6) * 6 + i;
        dma16c<L_RING + p * 4096, p * 32768>(w * 1024, wov, rs_wo, c * 128);
      }
    };
    wait_vm<3>();
    BARRIER();
    TS(1);
    sfor<(H ? 9 : 8)>([&](auto n) { rd(IC(0), n); });
    if constexpr (H) { stat_read(IC(0)); stat_read(IC(1)); }
    SCHED();
    sfor<16>([&](auto HS_) {
      constexpr int hs = HS_.value, s = hs / 2, kk = hs & 1;
      constexpr int nm = H ? 9 : 6;
      constexpr int f0 = hs <= 6 ? (hs * 10) / 7 : hs <= 12 ? 10 + ((hs - 7) * 10) / 6 : 20;
      constexpr int f1 = hs <= 6 ? ((hs + 1) * 10) / 7 : hs <= 12 ? 10 + ((hs - 6) * 10) / 6 : 20;
      sfor<nm>([&](auto N_) {
        constexpr int n = N_.value;
        mm(IC(kk), N_);
        SCHED();
        if constexpr (H) {
          if constexpr (hs < 15 && (!ABL_NOLDS || hs == 0)) rd(IC(hs + 1), N_);
          if constexpr (hs == 7 && n == 0) stat_read(IC(10));
          if constexpr (hs == 11 && n == 2) stat_out(IC(0));
          if constexpr (hs == 12 && (n == 2 || n == 5)) stat_out(IC(n == 2 ? 1 : 2));
        } else {
          if constexpr (hs < 15 && (!ABL_NOLDS || hs == 0)) {
            if constexpr (n < 2) { rd(IC(hs + 1), IC(2 * n)); rd(IC(hs + 1), IC(2 * n + 1)); }
            else rd(IC(hs + 1), IC(n + 2));
          }
          constexpr int B = (hs + 1) / 2;
          if constexpr (!ABL_NODMA) stream(IC(B), IC(hs == 0 || hs == 15 ? n : (n & 1) ? 6 + (1 - kk) * 3 + n / 2 : (1 - kk) * 3 + n / 2));
        }
        SCHED();
      });
      if constexpr (H) {
        sfor<f1 - f0>([&](auto I_) {
          constexpr int f = f0 + I_.value;
          if constexpr (!ABL_NOSTAT) stat_mm(IC(f));
          SCHED();
          if constexpr (f + 2 < 20 && f + 2 != 10 && f + 2 != 11) stat_read(IC(f + 2));
          if constexpr (f == 10) stat_read(IC(11));
          SCHED();
        });
      }
      if constexpr (H && hs == 12) { stat_out(IC(3)); stat_out(IC(4)); SCHED(); }
      if constexpr (hs == 14) {
        const u32 row = 16 + (tid >> 3), ch = c * 8 + (tid & 7);
        xres = *(LAS u32x4*)(L + L_X + (row >> 1) * 2048 + (row & 1) * 512 + (ch >> 5) * 1024 + (((ch & 31) ^ (row & 15)) << 4));
      }
      if constexpr (kk == 0) {
        constexpr int B = s + 1;
        if constexpr (B <= 7) TD(2 * B - 1);
        if constexpr (H) {
          if constexpr (B == 1) wait_vm<0>();
        } else {
          if constexpr (B < 8) wait_vm<(B == 2 || B == 3 ? 11 : 6)>();
        }
        if constexpr (hs == 12 || hs == 14) BARRIER();
        else {
          constexpr int nst = (f0 + 2 < 20 && f0 + 2 != 10 && f0 + 2 != 11 ? 1 : 0) + (f1 - f0 > 1 && f0 + 3 < 20 && f0 + 3 != 10 && f0 + 3 != 11 ? 1 : 0);
          asm volatile("s_waitcnt lgkmcnt(%0)\n\ts_barrier" ::"n"(H ? 2 + nst : 1) : "memory");
        }
        if constexpr (B <= 7) TD(2 * B);
        SCHED();
      }
      if constexpr (!H && hs == 13) {
        const u32 tok = hh * 20 + r16 + (q & 1) * 4;
        const f32x4 p0 = *(LAS f32x4*)(L + L_PART + tok * 32), p1 = *(LAS f32x4*)(L + L_PART + tok * 32 + 16);
        const float m = vadd(vadd(p0.x, p0.z), vadd(p1.x, p1.z)) * (1.0f / D);
        const float v = fmaxf(vadd(vadd(p0.y, p0.w), vadd(p1.y, p1.w)) * (1.0f / D) - m * m, 0.0f);
        *(LAS float*)(L + L_MR + tok * 4) = m;
        *(LAS float*)(L + L_MR + 384 + tok * 4) = __builtin_amdgcn_rsqf(v + EPS);
        SCHED();
      }
    });
    if constexpr (H) {
      const u32 ex = L_X + 65536 + hh * 2048 + lane * 16;
      *(LAS f32x4*)(L + ex) = aK[2];
      *(LAS f32x4*)(L + ex + 1024) = aV[2];
      if (r16 == 0) {
        *(LAS f32x4*)(L + L_CS + hh * 192 + q * 16) = aC;
        *(LAS f32x4*)(L + L_CS + hh * 192 + 64 + q * 16) = aK[0];
      }
      if (q == 0) *(LAS float*)(L + L_CS + hh * 192 + 128 + r16 * 4) = aV[0].x;
    }
  };
  if (heavy) p1_loop(std::true_type{}); else p1_loop(std::false_type{});
  __builtin_amdgcn_s_setprio(0);
  TS(2);
  if (w == 4) {
    const rsrc_t rs_b1 = __builtin_amdgcn_make_buffer_rsrc((void*)b1, 0, F * 2, 0x00020000);
    const rsrc_t rs_b2 = __builtin_amdgcn_make_buffer_rsrc((void*)b2, 0, S * 2, 0x00020000);
    dma4(L_B1, lane * 4, rs_b1, j * 128);
    dma4(L_B2, lane * 4, rs_b2, t * 128);
    __builtin_amdgcn_raw_buffer_store_b64(u32x2{0xffffffffu, 0xffffffffu}, rs_ws, lane * 8, WS_ST + g * 512, 16);
  }
  if (p3) w1_issue(0, stgA);
  BARRIER();
  {
    if (!heavy) {
      const u32 ex = L_X + 65536 + hh * 2048 + lane * 16;
      aK[0] = *(LAS f32x4*)(L + ex);
      aV[0] = *(LAS f32x4*)(L + ex + 1024);
    }
    const f32x4 csQ = *(LAS f32x4*)(L + L_CS + hh * 192 + q * 16), csK = *(LAS f32x4*)(L + L_CS + hh * 192 + 64 + q * 16);
    const float csV = *(LAS float*)(L + L_CS + hh * 192 + 128 + r16 * 4);
    const u32 mr = L_MR + ta * 128;
    float mL[3];
    f32x4 mR[3];
#pragma unroll
    for (int a = 0; a < 3; ++a) {
      mL[a] = *(LAS float*)(L + mr + a * 64 + r16 * 4);
      mR[a] = *(LAS f32x4*)(L + mr + a * 64 + q * 16);
    }
    const u32 k0 = 4 * q + (r16 & 1);
    const bool own0 = k0 <= r16, own1 = k0 + 2 <= r16;
    float rq[2], rk[2][2];
#pragma unroll
    for (int a = 0; a < 2; ++a) {
      rq[a] = *(LAS float*)(L + mr + 384 + (a + 1) * 64 + r16 * 4);
      rk[a][0] = *(LAS float*)(L + mr + 384 + a * 64 + (k0 + (own0 ? 16 : 0)) * 4);
      rk[a][1] = *(LAS float*)(L + mr + 384 + a * 64 + (k0 + 2 + (own1 ? 16 : 0)) * 4);
    }
    u32x2 qf[2], kf[3], vf[3];
#pragma unroll
    for (int a = 0; a < 2; ++a) {
      const f32x4 v = aQ[a] - mL[a + 1] * csQ;
      qf[a] = u32x2{pk(v[0], v[1]), pk(v[2], v[3])};
    }
#pragma unroll
    for (int a = 0; a < 3; ++a) {
      const f32x4 v = aK[a] - mL[a] * csK;
      kf[a] = u32x2{pk(v[0], v[1]), pk(v[2], v[3])};
    }
#pragma unroll
    for (int a = 0; a < 3; ++a) {
      const f32x4 v = aV[a] - mR[a] * csV;
      vf[a] = u32x2{pk(v[0], v[1]), pk(v[2], v[3])};
    }
    const bool odd = r16 & 1;
    float lg[2][2], mx[2];
#pragma unroll
    for (int a = 0; a < 2; ++a) {
      const f32x4 sp = mfma16(kf[a], qf[a], z4);
      const f32x4 so = mfma16(kf[a + 1], qf[a], z4);
      const float cq = rq[a] * (0.25f * 1.44269504088896f);
      const float ev0 = own0 ? so.x : sp.x, od0 = own0 ? so.y : sp.y;
      const float ev1 = own1 ? so.z : sp.z, od1 = own1 ? so.w : sp.w;
      lg[a][0] = (odd ? od0 : ev0) * (rk[a][0] * cq);
      lg[a][1] = (odd ? od1 : ev1) * (rk[a][1] * cq);
      if (a == 0) {
        const bool first = (t == 0) && heavy;
        lg[a][0] = (first && !own0) ? -1.44269504088896e9f : lg[a][0];
        lg[a][1] = (first && !own1) ? -1.44269504088896e9f : lg[a][1];
      }
      mx[a] = vmax(lg[a][0], lg[a][1]);
    }
    allred2<true>(mx[0], mx[1]);
    float ee[2][2], sm[2];
#pragma unroll
    for (int a = 0; a < 2; ++a) {
      ee[a][0] = __builtin_amdgcn_exp2f(lg[a][0] - mx[a]);
      ee[a][1] = __builtin_amdgcn_exp2f(lg[a][1] - mx[a]);
      sm[a] = ee[a][0] + ee[a][1];
    }
    allred2<false>(sm[0], sm[1]);
    u32x2 of[2];
    const u32 sel = odd ? 0x01000c0cu : 0x0c0c0100u, sel1 = odd ? 0x03020c0cu : 0x0c0c0302u;
#pragma unroll
    for (int a = 0; a < 2; ++a) {
      const float inv = __builtin_amdgcn_rcpf(sm[a]);
      const u32 pw = pk(ee[a][0] * rk[a][0] * inv, ee[a][1] * rk[a][1] * inv);
      const u32 p0 = __builtin_amdgcn_perm(0u, pw, sel), p1 = __builtin_amdgcn_perm(0u, pw, sel1);
      const u32x4 pf = {own0 ? 0u : p0, own1 ? 0u : p1, own0 ? p0 : 0u, own1 ? p1 : 0u};
      const u32x4 vv = {vf[a].x, vf[a].y, vf[a + 1].x, vf[a + 1].y};
      const f32x4 o = mfma32(vv, pf, z4);
      of[a] = u32x2{pk(o[0], o[1]), pk(o[2], o[3])};
    }
    auto s0 = __builtin_amdgcn_permlane16_swap(of[0].x, of[1].x, false, false);
    auto s1 = __builtin_amdgcn_permlane16_swap(of[0].y, of[1].y, false, false);
    const u32x4 ov = {s0[0], s1[0], s0[1], s1[1]};
    const u32 ooff = (q & 1) * 2048 + ((q >> 1) * 16 + r16) * 16;
    st_sc1(ov, rs_ws, ooff, WS_O + g * 8192 + ta * 4096 + hh * 512);
  }
  TS(3);
  wait_vm<0>();
  BARRIER();
  if (w == 0 && lane < 8) __builtin_amdgcn_raw_buffer_store_b64(__builtin_bit_cast(u32x2, tag), rs_ws, lane * 128, WS_F1 + t * 1024 + c * 8, 16);
  TS(4);

  u32x4 B1[8][4];
  u32x4 hreg;
  const u32 stg = stgB;
  auto w1_read = [&](int k, u32 lds) {
#pragma unroll
    for (int n = 0; n < 4; ++n) B1[k][n] = trfrag(L, lds + (trN ^ (n << 5)));
  };
  const u32 redw = (q * 16) * 16;
  const u32 s_ = tid >> 3, gq = tid & 7;
  const u32 redr = (((gq >> 1) * 4 + (s_ >> 4)) * 1024) + ((gq & 1) * 32 + ((s_ & 15) ^ (gq << 1))) * 16;
  {
    if (p3) w1_issue(1, stg);
    u32x4 Bo[4][4];
#pragma unroll
    for (int k = 0; k < 4; ++k)
#pragma unroll
      for (int n = 0; n < 4; ++n) Bo[k][n] = trfrag(L, L_RING + hh * 16384 + (trN ^ (n << 5)) + k * 4096);
    if (p3) w1_read(0, L_X + w * 4096);
    BARRIER();
    poll_tag(rs_ws, (lane & 1) * 8, WS_F1 + g * 128 + hh * 16, tag);
    TS(5);
    u32x4 ofr[2][4];
#pragma unroll
    for (int k = 0; k < 4; ++k)
#pragma unroll
      for (int tt = 0; tt < 2; ++tt)
        ofr[tt][k] = ld_sc1(rs_ws, lane * 16 + tt * 2048 + (k & 1) * 1024, WS_O + (t * 8 + 2 * hh + (k >> 1)) * 8192 + th * 4096);
    SCHED();
    f32x4 acc[2][4];
#pragma unroll
    for (int tt = 0; tt < 2; ++tt)
#pragma unroll
      for (int n = 0; n < 4; ++n) acc[tt][n] = f32x4{0, 0, 0, 0};
#pragma unroll
    for (int k = 0; k < 4; ++k)
#pragma unroll
      for (int tt = 0; tt < 2; ++tt)
#pragma unroll
        for (int n = 0; n < 4; ++n) acc[tt][n] = mfma32(Bo[k][n], ofr[tt][k], acc[tt][n]);
    SCHED();
    if (p3) { w1_issue(2, L_X + w * 4096); }
#pragma unroll
    for (int n = 0; n < 4; ++n)
#pragma unroll
      for (int tt = 0; tt < 2; ++tt)
        *(LAS f32x4*)(L + L_RING + hh * 16384 + (n * 4 + th * 2 + tt) * 1024 + redw + ((r16 ^ ((2 * n + (q >> 1)) << 1)) << 4)) = acc[tt][n];
    BARRIER();
    TS(6);
    f32x4 a0 = *(LAS f32x4*)(L + L_RING + redr), a1 = *(LAS f32x4*)(L + L_RING + redr + 256);
#pragma unroll
    for (int k = 1; k < 4; ++k) {
      a0 += *(LAS f32x4*)(L + L_RING + k * 16384 + redr);
      a1 += *(LAS f32x4*)(L + L_RING + k * 16384 + redr + 256);
    }
    hreg = u32x4{pk(blo(xres.x) + a0[0], bhi(xres.x) + a0[1]), pk(blo(xres.y) + a0[2], bhi(xres.y) + a0[3]),
                 pk(blo(xres.z) + a1[0], bhi(xres.z) + a1[1]), pk(blo(xres.w) + a1[2], bhi(xres.w) + a1[3])};
    const f32x4 h0 = {blo(hreg.x), bhi(hreg.x), blo(hreg.y), bhi(hreg.y)}, h1 = {blo(hreg.z), bhi(hreg.z), blo(hreg.w), bhi(hreg.w)};
    const f32x4 hs = h0 + h1, hq = h0 * h0 + h1 * h1;
    const float ps = sum8((hs.x + hs.y) + (hs.z + hs.w)), pq = sum8((hq.x + hq.y) + (hq.z + hq.w));
    if (gq == 0) __builtin_amdgcn_raw_buffer_store_b64(u32x2{min(f2u(ps), 0xfffffffeu), min(f2u(pq), 0xfffffffeu)}, rs_ws, s_ * 8, WS_ST + g * 512, 16);
    TS(7);
    if (p3) {
      w1_read(1, stg);
      SCHED();
      WAIT_LGKM0();
      SCHED();
      w1_issue(3, stg);
    }
    u32x2 sv;
    for (int sp = 0; sp < SPIN_CAP; ++sp) {
      FENCE();
      sv = __builtin_amdgcn_raw_buffer_load_b64(rs_ws, gq * 512 + s_ * 8, WS_ST + t * 4096, 16);
      if (!__any(max(sv.x, sv.y) == 0xffffffffu)) break;
    }
    TS(8);
    {
      const float m = sum8(u2f(sv.x)) * (1.0f / D);
      const float v = fmaxf(sum8(u2f(sv.y)) * (1.0f / D) - m * m, 0.0f);
      const float r1 = __builtin_amdgcn_rsqf(v + EPS);
      const float r2 = __builtin_amdgcn_rsqf(v * r1 * r1 + EPS);
      const float r = r1 * r2;
      const f32x4 n0 = (h0 - m) * r, n1 = (h1 - m) * r;
      const u32x4 hn = {pk(n0[0], n0[1]), pk(n0[2], n0[3]), pk(n1[0], n1[1]), pk(n1[2], n1[3])};
      const u32 ta = L_RING + 65536 + w * 1024;
      *(LAS u32x4*)(L + ta + ((lane * 16) ^ (((lane >> 4) & 1) << 6))) = hn;
      FENCE();
      const u32x4 hf = trfrag(L, ta + qp * 128 + ((q * 32) ^ ((qp >> 1) << 6)) + pp * 8);
      st_sc1(hf, rs_ws, q * 1024 + r16 * 16, WS_HN + c * 262144 + (2 * t + th) * 4096 + hh * 256);
    }
    wait_vm<0>();
    BARRIER();
    if (w == 0 && lane < 32) __builtin_amdgcn_raw_buffer_store_b64(__builtin_bit_cast(u32x2, tag), rs_ws, lane * 2048, WS_F2 + c * 256 + t * 8, 16);
    TS(9);
  }

  const u32 w2E = (lane >> 3) * (S * 2) + swzE, w2O = (lane >> 3) * (S * 2) + (swzE ^ 64) + 8 * (S * 2) - 1024;
  const u32 w2s = w * 224 * (S * 2) + t * 128;
  if (p3) {
    w1_issue(4, L_RING + w * 8192);
    w1_issue(5, L_RING + w * 8192 + 4096);
    w1_read(2, L_X + w * 4096);
    w1_read(3, stg);
    SCHED();
    WAIT_LGKM0();
    SCHED();
    w1_issue(6, L_X + w * 4096);
    w1_issue(7, stg);
    wait_vm<12>();
    w1_read(4, L_RING + w * 8192);
    wait_vm<8>();
    w1_read(5, L_RING + w * 8192 + 4096);
    wait_vm<4>();
    w1_read(6, L_X + w * 4096);
    wait_vm<0>();
    w1_read(7, stg);
    const u32 mte = w >> 1, kp = w & 1;
    const u32x4 bb = *(LAS u32x4*)(L + L_B1 + kp * 64 + q * 16);
    BARRIER();
    poll_tag(rs_ws, (lane & 3) * 8, WS_F2 + g * 256 + w * 32, tag);
    TS(10);
    constexpr int RING = RING3;
    u32x4 A[RING];
    const u32 as = WS_HN + c * 262144 + w * 32768;
    u32 ak[8];
#pragma unroll
    for (int k = 0; k < 8; ++k) ak[k] = as + ((k + rot1) & 7) * 4096;
#pragma unroll
    for (int i = 0; i < RING; ++i) A[i] = ld_sc1(rs_ws, lane * 16 + (i >> 3) * 1024, ak[i & 7]);
    SCHED();
#pragma unroll
    for (int mt = 0; mt < 4; ++mt) {
      f32x4 acc[4];
#pragma unroll
      for (int n = 0; n < 4; ++n) acc[n] = f32x4{0, 0, 0, 0};
#pragma unroll
      for (int k = 0; k < 8; ++k) {
        const int i = mt * 8 + k;
        const u32x4 cur = A[i % RING];
#pragma unroll
        for (int n = 0; n < 4; ++n) acc[n] = mfma32(B1[k][n], cur, acc[n]);
        if (i + RING < 32) A[i % RING] = ld_sc1(rs_ws, lane * 16 + ((i + RING) >> 3) * 1024, ak[(i + RING) & 7]);
        SCHED();
      }
#pragma unroll
      for (int n = 0; n < 4; ++n) *(LAS f32x4*)(L + ((w * 4 + mt) * 4 + n) * 1024 + lane * 16) = acc[n];
    }
    BARRIER();
    TS(11);
    if (g == 0 && tid == 0) *(GAS u64*)(ws + WS_EPOCH) = epoch + 1;
    {
      f32x4 s0 = {blo(bb.x), bhi(bb.x), blo(bb.y), bhi(bb.y)}, s1 = {blo(bb.z), bhi(bb.z), blo(bb.w), bhi(bb.w)};
      const u32 ra = (mte * 4 + 2 * kp + (q >> 1)) * 1024 + ((q & 1) * 32 + r16) * 16;
#pragma unroll
      for (int ww = 0; ww < 8; ++ww) {
        s0 += *(LAS f32x4*)(L + ww * 16384 + ra);
        s1 += *(LAS f32x4*)(L + ww * 16384 + ra + 256);
      }
      float v[8] = {s0[0], s0[1], s0[2], s0[3], s1[0], s1[1], s1[2], s1[3]};
#pragma unroll
      for (int e = 0; e < 8; ++e) {
        const float z = v[e] * (v[e] * v[e] * (-0.044715f * 2.302208198f) - 2.302208198f);
        v[e] = v[e] * __builtin_amdgcn_rcpf(1.0f + __builtin_amdgcn_exp2f(z));
      }
      const u32x4 uf = {pk(v[0], v[1]), pk(v[2], v[3]), pk(v[4], v[5]), pk(v[6], v[7])};
      st_sc1(uf, rs_ws, lane * 16, WS_U + c * (56 * 4096) + (2 * j + kp) * 4096 + mte * 1024);
    }
    wait_vm<0>();
    BARRIER();
    if (w == 0 && lane < 32) __builtin_amdgcn_raw_buffer_store_b64(__builtin_bit_cast(u32x2, tag), rs_ws, lane * 2048, WS_F3 + c * 256 + j * 8, 16);
  } else {
    const u32 tv = (lane >> 3) * (S * 2) + (lane & 7) * 128;
#pragma unroll
    for (int i = 0; i < 7; ++i) dma4(L_SCR, tv, rs_w2, (((c & 3) * 8 + w) * 7 + i) * 8 * (S * 2) + sq * 1024);
    wait_vm<0>();
  }
  TS(12);

  {
    u32x4 B2[7][4];
    auto w2_issue = [&](int k, u32 lds) { dma16x4<8 * S * 2>(lds, w2E, w2O, rs_w2, w2s + k * 32 * (S * 2)); };
    auto w2_read = [&](int k, u32 lds) {
#pragma unroll
      for (int n = 0; n < 4; ++n) B2[k][n] = trfrag(L, lds + (trN ^ (n << 5)));
    };
#pragma unroll
    for (int k = 0; k < 4; ++k) w2_issue(k, w * 16384 + k * 4096);
#pragma unroll
    for (int k = 0; k < 4; ++k) {
      wait_vm<12>();
      w2_read(k, w * 16384 + k * 4096);
      if (k < 3) {
        SCHED();
        WAIT_LGKM0();
        SCHED();
        w2_issue(4 + k, w * 16384 + k * 4096);
      }
    }
    wait_vm<8>();
    w2_read(4, w * 16384);
    wait_vm<4>();
    w2_read(5, w * 16384 + 4096);
    wait_vm<0>();
    w2_read(6, w * 16384 + 8192);
    const float bias = blo((u32) * (LAS u16*)(L + L_B2 + s_ * 2));
    BARRIER();
    TS(13);
    poll_tag(rs_ws, (lane & 3) * 8, WS_F3 + g * 256 + ((7 * w) >> 1) * 8, tag);
    TS(14);
    constexpr int RING = RING4;
    u32x4 A[RING];
    const u32 as = WS_U + c * (56 * 4096) + w * (7 * 4096);
#pragma unroll
    for (int i = 0; i < RING; ++i) A[i] = ld_sc1(rs_ws, lane * 16 + (i / 7) * 1024, as + (i % 7) * 4096);
    SCHED();
#pragma unroll
    for (int mt = 0; mt < 4; ++mt) {
      f32x4 acc[4];
#pragma unroll
      for (int n = 0; n < 4; ++n) acc[n] = f32x4{0, 0, 0, 0};
#pragma unroll
      for (int k = 0; k < 7; ++k) {
        const int i = mt * 7 + k;
        const u32x4 cur = A[i % RING];
#pragma unroll
        for (int n = 0; n < 4; ++n) acc[n] = mfma32(cur, B2[k][n], acc[n]);
        if (i + RING < 28) A[i % RING] = ld_sc1(rs_ws, lane * 16 + ((i + RING) / 7) * 1024, as + ((i + RING) % 7) * 4096);
        SCHED();
      }
      const u32 wl = redw + ((r16 ^ ((2 * mt + (q >> 1)) << 1)) << 4);
#pragma unroll
      for (int n = 0; n < 4; ++n) *(LAS f32x4*)(L + ((w * 4 + mt) * 4 + n) * 1024 + wl) = acc[n];
    }
    BARRIER();
    {
      f32x4 s0 = {blo(hreg.x) + bias, bhi(hreg.x) + bias, blo(hreg.y) + bias, bhi(hreg.y) + bias};
      f32x4 s1 = {blo(hreg.z) + bias, bhi(hreg.z) + bias, blo(hreg.w) + bias, bhi(hreg.w) + bias};
#pragma unroll
      for (int ww = 0; ww < 8; ++ww) {
        s0 += *(LAS f32x4*)(L + ww * 16384 + redr);
        s1 += *(LAS f32x4*)(L + ww * 16384 + redr + 256);
      }
      const u32x4 ov = {pk(s0[0], s0[1]), pk(s0[2], s0[3]), pk(s1[0], s1[1]), pk(s1[2], s1[3])};
      *(GAS u32x4*)(out + (size_t)(t * 64 + s_) * D + c * 64 + gq * 8) = ov;
    }
  }
  asm volatile(".Lfb_end_mark:" ::: "memory");
}

extern "C" void kernel_launch(void* const* d_in, const int* in_sizes, int n_in, void* d_out, int out_size, void* d_ws, size_t ws_size,
                              hipStream_t stream) {
  (void)in_sizes; (void)n_in; (void)out_size; (void)ws_size;
  (void)hipFuncSetAttribute((const void*)fused_block, hipFuncAttributeMaxDynamicSharedMemorySize, L_TOTAL);
  fused_block<<<256, 512, L_TOTAL, stream>>>((const u16*)d_in[0], (const u16*)d_in[1], (char*)d_ws, (const u16*)d_in[2], (const u16*)d_in[4],
                                             (const u16*)d_in[6], (const u16*)d_in[5], (const u16*)d_in[7], (u16*)d_out);
}
```

```cpp
#define FIXT 6000
#define OUT_AUX 16
#include <hip/hip_runtime.h>
#include <utility>
#pragma clang diagnostic ignored "-Wint-to-pointer-cast"

typedef unsigned short u16;
typedef unsigned u32;
typedef unsigned long long u64;
typedef __bf16 bf16x8 __attribute__((ext_vector_type(8)));
typedef __bf16 bf16x2 __attribute__((ext_vector_type(2)));
typedef short s16x4 __attribute__((ext_vector_type(4)));
typedef float f32x4 __attribute__((ext_vector_type(4)));
typedef float f32x2 __attribute__((ext_vector_type(2)));
typedef u32 u32x4 __attribute__((ext_vector_type(4)));
typedef u32 u32x2 __attribute__((ext_vector_type(2)));
#define LAS __attribute__((address_space(3)))
#define GAS __attribute__((address_space(1)))
#define DI __device__ __forceinline__
#define RLX_AGENT __ATOMIC_RELAXED, __HIP_MEMORY_SCOPE_AGENT
typedef __amdgpu_buffer_rsrc_t rsrc_t;

#define ABL_NOSTAT 0
#define ABL_NODMA 0
#define ABL_NOLDS 0
#define STAMP_TID 0
#define RING3 6
#define RING4 6
#define NT ""

constexpr int S = 2048, D = 512, F = 1792;
constexpr float EPS = 1e-5f;
constexpr u32 WS_EPOCH = 0;
constexpr u32 WS_F1 = 4096;
constexpr u32 WS_F2 = WS_F1 + 256 * 128;
constexpr u32 WS_F3 = WS_F2 + 256 * 256;
constexpr u32 WS_ST = WS_F3 + 256 * 256;
constexpr u32 WS_O = 1u << 20;
constexpr u32 WS_HN = WS_O + (2u << 20);
constexpr u32 WS_U = WS_HN + (2u << 20);
constexpr u32 WS_END = WS_U + (2u << 20);
constexpr u32 WS_STAMP = WS_END;
constexpr u32 L_RING = 0;
constexpr u32 L_X = 73728;
constexpr u32 L_MISC = 155648;
constexpr u32 L_PART = L_MISC;
constexpr u32 L_MR = L_MISC + 5120;
constexpr u32 L_ONES = L_MISC + 5888;
constexpr u32 L_CS = L_MISC + 4096;
constexpr u32 L_SCR = L_MISC + 6144;
constexpr u32 L_B1 = L_MISC + 7680;
constexpr u32 L_B2 = L_MISC + 7936;
constexpr u32 L_TOTAL = 163840;
constexpr int SPIN_CAP = 1 << 18;

DI void dma16(u32 lds, u32 voff, rsrc_t rs, u32 soff) {
  asm volatile("s_mov_b32 m0, %0\n\ts_nop 0\n\tbuffer_load_dwordx4 %1, %2, %3 offen" NT " lds" ::"s"(lds), "v"(voff), "s"(rs), "s"(soff) : "memory");
}
DI void dma4(u32 lds, u32 voff, rsrc_t rs, u32 soff) {
  asm volatile("s_mov_b32 m0, %0\n\ts_nop 0\n\tbuffer_load_dword %1, %2, %3 offen" NT " lds" ::"s"(lds), "v"(voff), "s"(rs), "s"(soff) : "memory");
}
template <int LC, int SC>
DI void dma16c(u32 lds, u32 voff, rsrc_t rs, u32 soff) {
  u32 tmp;
  asm volatile("s_add_u32 m0, %1, %5\n\ts_add_u32 %0, %4, %6\n\tbuffer_load_dwordx4 %2, %3, %0 offen" NT " lds"
               : "=&s"(tmp) : "s"(lds), "v"(voff), "s"(rs), "s"(soff), "n"(LC), "n"(SC) : "memory", "scc");
}
template <int STEP>
DI void dma16x4(u32 lds, u32 voffE, u32 voffO, rsrc_t rs, u32 soff) {
  u32 tmp;
  asm volatile(
      "s_mov_b32 m0, %1\n\t"
      "s_add_u32 %0, %5, %6\n\t"
      "buffer_load_dwordx4 %2, %4, %5 offen" NT " lds\n\t"
      "buffer_load_dwordx4 %3, %4, %5 offen offset:1024" NT " lds\n\t"
      "buffer_load_dwordx4 %2, %4, %0 offen offset:2048" NT " lds\n\t"
      "buffer_load_dwordx4 %3, %4, %0 offen offset:3072" NT " lds"
      : "=&s"(tmp)
      : "s"(lds), "v"(voffE), "v"(voffO), "s"(rs), "s"(soff), "n"(2 * STEP - 2048)
      : "memory", "scc");
}
template <int N> DI void wait_vm() { asm volatile("s_waitcnt vmcnt(%0)" ::"n"(N) : "memory"); }
#define WAIT_LGKM0() asm volatile("s_waitcnt lgkmcnt(0)" ::: "memory")
#define BARRIER() asm volatile("s_waitcnt lgkmcnt(0)\n\ts_barrier" ::: "memory")
#define SCHED() __builtin_amdgcn_sched_barrier(0)
#define FENCE() asm volatile("" ::: "memory")

DI f32x4 mfma32(u32x4 a, u32x4 b, f32x4 c) {
  return __builtin_amdgcn_mfma_f32_16x16x32_bf16(__builtin_bit_cast(bf16x8, a), __builtin_bit_cast(bf16x8, b), c, 0, 0, 0);
}
DI f32x4 mfma16(u32x2 a, u32x2 b, f32x4 c) {
  return __builtin_amdgcn_mfma_f32_16x16x16bf16_1k(__builtin_bit_cast(s16x4, a), __builtin_bit_cast(s16x4, b), c, 0, 0, 0);
}
DI u32x2 trd(u32 L, u32 addr) {
  return __builtin_bit_cast(u32x2, __builtin_amdgcn_ds_read_tr16_b64_v4i16((LAS s16x4*)(L + addr)));
}
DI u32x4 trfrag(u32 L, u32 a) {
  u32x2 lo = trd(L, a), hi = trd(L, a + 512);
  return u32x4{lo.x, lo.y, hi.x, hi.y};
}
DI u32 pk(float a, float b) {
  f32x2 v = {a, b};
  return __builtin_bit_cast(u32, __builtin_convertvector(v, bf16x2));
}
DI float blo(u32 v) { return __builtin_bit_cast(float, v << 16); }
DI float bhi(u32 v) { return __builtin_bit_cast(float, v & 0xffff0000u); }
DI u32 f2u(float f) { return __builtin_bit_cast(u32, f); }
DI float u2f(u32 u) { return __builtin_bit_cast(float, u); }
DI float vmax(float a, float b) { float r; asm("v_max_f32 %0, %1, %2" : "=v"(r) : "v"(a), "v"(b)); return r; }
DI float vadd(float a, float b) { float r; asm("v_add_f32 %0, %1, %2" : "=v"(r) : "v"(a), "v"(b)); return r; }
DI float red4(float a, float b, float c, float d) {
  auto r1 = __builtin_amdgcn_permlane16_swap(f2u(a), f2u(b), false, false);
  auto r2 = __builtin_amdgcn_permlane16_swap(f2u(c), f2u(d), false, false);
  float ab = vadd(u2f(r1[0]), u2f(r1[1])), cd = vadd(u2f(r2[0]), u2f(r2[1]));
  auto r3 = __builtin_amdgcn_permlane32_swap(f2u(ab), f2u(cd), false, false);
  return vadd(u2f(r3[0]), u2f(r3[1]));
}
template <bool MAX>
DI void allred2(float& a, float& b) {
  auto r1 = __builtin_amdgcn_permlane16_swap(f2u(a), f2u(b), false, false);
  float t = MAX ? vmax(u2f(r1[0]), u2f(r1[1])) : vadd(u2f(r1[0]), u2f(r1[1]));
  auto r2 = __builtin_amdgcn_permlane32_swap(f2u(t), f2u(t), false, false);
  float u = MAX ? vmax(u2f(r2[0]), u2f(r2[1])) : vadd(u2f(r2[0]), u2f(r2[1]));
  auto r3 = __builtin_amdgcn_permlane16_swap(f2u(u), f2u(u), false, false);
  a = u2f(r3[0]);
  b = u2f(r3[1]);
}
template <int CTRL> DI float dppadd(float v) { return v + __builtin_amdgcn_update_dpp(0.0f, v, CTRL, 0xf, 0xf, true); }
DI float sum8(float v) {
  v = dppadd<0xB1>(v);
  v = dppadd<0x4E>(v);
  v = dppadd<0x141>(v);
  return v;
}
template <int HI> DI void stat3(float& tmp, float& s, float& q, u32 v) {
  if constexpr (HI)
    asm volatile("v_and_b32 %0, 0xffff0000, %3\n\tv_add_f32 %1, %1, %0\n\tv_fmac_f32 %2, %0, %0" : "+v"(tmp), "+v"(s), "+v"(q) : "v"(v));
  else
    asm volatile("v_lshlrev_b32 %0, 16, %3\n\tv_add_f32 %1, %1, %0\n\tv_fmac_f32 %2, %0, %0" : "+v"(tmp), "+v"(s), "+v"(q) : "v"(v));
}
DI u32x4 ld_sc1(rsrc_t rs, u32 voff, u32 soff) { return __builtin_amdgcn_raw_buffer_load_b128(rs, voff, soff, 16); }
DI void st_sc1(u32x4 v, rsrc_t rs, u32 voff, u32 soff) { __builtin_amdgcn_raw_buffer_store_b128(v, rs, voff, soff, 16); }

DI void poll_tag(rsrc_t rs, u32 voff, u32 soff, u64 tag) {
  for (int sp = 0; sp < SPIN_CAP; ++sp) {
    FENCE();
    const u32x2 f = __builtin_amdgcn_raw_buffer_load_b64(rs, voff, soff, 16);
    if (__all(__builtin_bit_cast(u64, f) == tag)) break;
  }
  FENCE();
}
template <typename Fn, int... I> DI void sfor_impl(Fn&& f, std::integer_sequence<int, I...>) { (f(std::integral_constant<int, I>{}), ...); }
template <int N, typename Fn> DI void sfor(Fn&& f) { sfor_impl(f, std::make_integer_sequence<int, N>{}); }

#define TS(i)
#define TD(i)

__global__ __launch_bounds__(512, 2) void fused_block(const u16* __restrict__ x, const u16* __restrict__ wqkv, char* __restrict__ ws,
                                                      const u16* __restrict__ wo, const u16* __restrict__ w1, const u16* __restrict__ w2,
                                                      const u16* __restrict__ b1, const u16* __restrict__ b2, u16* __restrict__ out) {
  constexpr u32 L = 0;
  TS(0);
  const u64 fix0 = __builtin_amdgcn_s_memrealtime();
  const u32 tid = threadIdx.x, lane = tid & 63;
  const u32 w = __builtin_amdgcn_readfirstlane(tid >> 6);
  const u32 b = blockIdx.x, xcd = b & 7, idx = b >> 3;
  if (w == 7) {
    u64 pc;
    u32 nbytes;
    asm volatile("s_getpc_b64 %0\n.Lfb_pc_mark:\n\ts_mov_b32 %1, .Lfb_end_mark-.Lfb_pc_mark" : "=s"(pc), "=s"(nbytes));
    const rsrc_t rs_pc = __builtin_amdgcn_make_buffer_rsrc((void*)(pc & ~(u64)15), 0, nbytes, 0x00020000);
    asm volatile("s_mov_b32 m0, %0\n\ts_nop 0\n\tbuffer_load_dwordx4 %1, %2, %3 offen lds" ::"s"(L_SCR), "v"(lane * 16), "s"(rs_pc), "s"(idx * 1024) : "memory");
  }
  const u32 c = (xcd & 1) * 4 + (idx & 3), tl = idx >> 2, sq = xcd >> 1, t = sq * 8 + tl;
  const u32 g = t * 8 + c;
  const bool p3 = tl < 7;
  const u32 j = sq * 7 + (p3 ? tl : 0);
  const u32 r16 = lane & 15, q = lane >> 4;
  const u32 hh = w & 3, th = w >> 2;

  const rsrc_t rs_x = __builtin_amdgcn_make_buffer_rsrc((void*)x, 0, S * D * 2, 0x00020000);
  const rsrc_t rs_wqkv = __builtin_amdgcn_make_buffer_rsrc((void*)wqkv, 0, D * 3 * D * 2, 0x00020000);

  const u32 hA = ((tl >> 2) ^ c) & 1;
  const u32 xrw = 2 * w + (lane >> 5);
  const u32 xchunk = ((lane & 31) ^ xrw) << 4;
  const u32 xv = xrw * 1024 + xchunk;
  const u32 xv0 = t == 0 ? xchunk : xv;
  const u32 xs0 = t == 0 ? 0 : t * 65536 - 16384;
  const u32 swzE = ((lane & 7) ^ (((lane >> 4) & 1) << 1)) << 4;
  const u32 swzW = swzE ^ ((w & 1) << 6);
  const u32 wv = (w * 8 + (lane >> 3)) * 3072 + swzW;
  const u32 wdst = (w >> 2) * 12288 + (w & 3) * 1024;
  auto stage_of = [&](u32 s) { return ((hA ^ (s >> 2)) << 2) | ((s + tl) & 3); };
  const u32 xlA = w * 2048 + hA * 1024, xsA = t * 65536 + hA * 512;
  auto dma_x = [&](auto A_, auto H_) {
    constexpr int a = A_.value, h = H_.value;
    if constexpr (a == 0) dma16c<L_X, 0>(xlA ^ (h * 1024), xv0, rs_x, xs0 + ((hA ^ h) * 512));
    else dma16c<L_X + a * 16384, (a - 1) * 16384>(xlA ^ (h * 1024), xv, rs_x, xsA ^ (h * 512));
  };
  auto dma_w = [&](auto S_, auto G_) {
    constexpr int s = S_.value, seg = G_.value;
    dma16c<L_RING + ((s + 1) % 3) * 24576 + seg * 4096, seg * 1024>(wdst, wv, rs_wqkv, stage_of(s) * (64 * 3072) + c * 128);
  };
#define IC(n) std::integral_constant<int, (n)>{}
  asm volatile("s_nop 4" ::: "memory");
  sfor<5>([&](auto a) { dma_x(a, IC(0)); });
  sfor<3>([&](auto s) { dma_w(IC(0), s); });
  sfor<3>([&](auto s) { dma_w(IC(1), s); });

  const rsrc_t rs_wo = __builtin_amdgcn_make_buffer_rsrc((void*)wo, 0, D * D * 2, 0x00020000);
  const rsrc_t rs_w1 = __builtin_amdgcn_make_buffer_rsrc((void*)w1, 0, S * F * 2, 0x00020000);
  const rsrc_t rs_w2 = __builtin_amdgcn_make_buffer_rsrc((void*)w2, 0, F * S * 2, 0x00020000);
  const rsrc_t rs_ws = __builtin_amdgcn_make_buffer_rsrc((void*)ws, 0, WS_END, 0x00020000);
  u64 epoch;
  asm volatile("s_load_dwordx2 %0, %1, 0x0\n\ts_waitcnt lgkmcnt(0)" : "=s"(epoch) : "s"(ws));
  const u64 tag = (epoch + 1) * 0x9E3779B97F4A7C15ull + 0x632BE59BD9B4E019ull;
  *(LAS u32*)(L + L_ONES + (lane & 3) * 4) = 0x3f803f80u;

  const bool heavy = w >= 4;
  const u32 ta = heavy ? 0u : 1u;
  f32x4 aQ[2], aK[3], aV[3], aC;
  const f32x4 z4 = {0, 0, 0, 0};
  aC = z4;
#pragma unroll
  for (int i = 0; i < 2; ++i) aQ[i] = z4;
#pragma unroll
  for (int i = 0; i < 3; ++i) { aK[i] = z4; aV[i] = z4; }
  const u32 qp = (lane & 15) >> 2, pp = lane & 3;
  const u32 fN = ((qp >> 1) & 1) | ((q & 1) << 1);
  const u32 trN = (8 * q + qp) * 128 + (fN << 5) + 8 * pp;
  const u32 wr = trN ^ (hh << 5);
  const u32 xl = L_X + (r16 >> 1) * 2048 + (r16 & 1) * 512 + ((q ^ r16) << 4);
  u32x4 xres;
  const u32 w1E = (lane >> 3) * (F * 2) + swzE, w1O = (lane >> 3) * (F * 2) + (swzE ^ 64) + 8 * (F * 2) - 1024;
  const u32 rot1 = 2 * (c & 3);
  const u32 w1s = w * 256 * (F * 2) + j * 128;
  auto w1_issue = [&](int k, u32 lds) { dma16x4<8 * F * 2>(lds, w1E, w1O, rs_w1, w1s + ((k + rot1) & 7) * 32 * (F * 2)); };
  const u32 stgA = L_X + w * 4096, stgB = L_X + 32768 + w * 4096;

  auto p1_loop = [&](auto H_) {
    constexpr bool H = H_.value;
    u32x4 Xf[2][3], Wf[2][3], Sf[2];
    f32x4 sS[5], sQ[5];
    if constexpr (H) {
#pragma unroll
      for (int i = 0; i < 5; ++i) { sS[i] = z4; sQ[i] = z4; }
    }
    u32x4 ones = {0x3f803f80u, 0x3f803f80u, 0x3f803f80u, 0x3f803f80u};
    asm volatile("" : "+v"(ones));
    const u32 xb1 = xl + (H ? 1 : 3) * 16384;
    const u32 xb0 = r16 == 0 ? L_ONES : xl;
    const u32 xm0 = r16 == 0 ? 0u : ~0u;
    const u32 xst = xl ^ (hh << 6);
    const u32 xv2 = (xv ^ 128) + 8192, xv02 = t == 0 ? (xchunk ^ 128) : xv2;
    const u32 wov = (w * 8 + (lane >> 3)) * 1024 + swzW;
    u32 xa1, xa0, wr2 = wr + 32768;
    asm volatile("" : "+v"(wr2));
    u32x2 wh;
    auto rd = [&](auto HS_, auto N_) {
      constexpr int s = HS_.value / 2, kk = HS_.value & 1, n = N_.value;
      constexpr u32 so = L_RING + ((s + 1) % 3) * 24576 + kk * 12288;
      if constexpr (n == 0) {
        const u32 sg = stage_of(s);
        const u32 sb = ((sg >> 2) << 10) | ((sg & 3) << 7) | (kk << 6);
        xa1 = xb1 ^ sb;
        if constexpr (H) xa0 = __builtin_amdgcn_bitop3_b32(xb0, sb, xm0, 0x78);
        Xf[kk][1] = *(LAS u32x4*)(L + xa1);
      }
      if constexpr (n >= 1 && n <= 6) {
        constexpr int seg = (n - 1) / 2;
        constexpr u32 o = so + seg * 4096 + ((n - 1) & 1) * 512;
        const u32x2 v = o < 65536 ? trd(L, wr + o) : trd(L, wr2 + (o - 32768));
        if constexpr (n & 1) wh = v;
        else Wf[kk][seg] = u32x4{wh.x, wh.y, v.x, v.y};
      }
      if constexpr (n == 7) Xf[kk][2] = *(LAS u32x4*)(L + xa1 + 16384);
      if constexpr (n == 8) Xf[kk][0] = *(LAS u32x4*)(L + xa0);
    };
    auto mm = [&](auto KK_, auto N_) {
      constexpr int kk = KK_.value, n = N_.value;
      if constexpr (n == 0) aQ[0] = mfma32(Wf[kk][0], Xf[kk][1], aQ[0]);
      if constexpr (n == 1) aK[1] = mfma32(Wf[kk][1], Xf[kk][1], aK[1]);
      if constexpr (n == 2) aV[1] = mfma32(Xf[kk][1], Wf[kk][2], aV[1]);
      if constexpr (n == 3) aQ[1] = mfma32(Wf[kk][0], Xf[kk][2], aQ[1]);
      if constexpr (n == 4) aK[2] = mfma32(Wf[kk][1], Xf[kk][2], aK[2]);
      if constexpr (n == 5) aV[2] = mfma32(Xf[kk][2], Wf[kk][2], aV[2]);
      if constexpr (n == 6) aK[0] = mfma32(Wf[kk][1], Xf[kk][0], aK[0]);
      if constexpr (n == 7) aV[0] = mfma32(Xf[kk][0], Wf[kk][2], aV[0]);
      if constexpr (n == 8) aC = mfma32(Wf[kk][0], Xf[kk][0], aC);
    };
    auto stat_read = [&](auto F_) {
      constexpr int f = F_.value;
      Sf[f & 1] = *(LAS u32x4*)(L + ((xst ^ (((f / 10) ^ hA) << 10) ^ (((f / 5) & 1) << 8)) + (f % 5) * 16384));
    };
    auto stat_mm = [&](auto F_) {
      constexpr int f = F_.value;
      sS[f % 5] = mfma32(ones, Sf[f & 1], sS[f % 5]);
      sQ[f % 5] = mfma32(Sf[f & 1], Sf[f & 1], sQ[f % 5]);
    };
    const u32 spa = q == qp ? L_PART + r16 * 32 + hh * 8 : L_SCR - 2048 + 2048;
    const u32 sps = q == qp ? 512u : 0u;
    auto stat_out = [&](auto A_) {
      constexpr int a = A_.value;
      const bool b0 = r16 & 1, b1 = r16 & 2;
      const float d = b1 ? (b0 ? sQ[a].w : sQ[a].z) : (b0 ? sQ[a].y : sQ[a].x);
      *(LAS f32x2*)(L + spa + a * sps) = f32x2{sS[a].x, d};
    };
    auto stream = [&](auto B_, auto I_) {
      constexpr int B = B_.value, i = I_.value;
      if constexpr (B <= 5 && i < 6) {
        constexpr int s = B + 2, seg = i % 3, hi = i / 3;
        dma16c<L_RING + ((s + 1) % 3) * 24576 + hi * 12288 + seg * 4096, seg * 1024 + hi * 32 * 3072>(wdst, wv, rs_wqkv, stage_of(s) * (64 * 3072) + c * 128);
      } else if constexpr ((B == 1 || B == 2) && i < 11) {
        constexpr int a = i - 6, hi = B - 1;
        if constexpr (a == 0) dma16c<L_X + hi * 8192, 0>(xlA ^ 1024, hi ? xv02 : xv0, rs_x, xs0 + ((hA ^ 1) * 512));
        else dma16c<L_X + hi * 8192 + a * 16384, (a - 1) * 16384>(xlA ^ 1024, hi ? xv2 : xv, rs_x, xsA ^ 512);
      } else if constexpr (B >= 6 && i < (B == 8 ? 4 : 6)) {
        constexpr int p = (B - 6) * 6 + i;
        dma16c<L_RING + p * 4096, p * 32768>(w * 1024, wov, rs_wo, c * 128);
      }
    };
    wait_vm<3>();
    BARRIER();
    TS(1);
    sfor<(H ? 9 : 8)>([&](auto n) { rd(IC(0), n); });
    if constexpr (H) { stat_read(IC(0)); stat_read(IC(1)); }
    SCHED();
    sfor<16>([&](auto HS_) {
      constexpr int hs = HS_.value, s = hs / 2, kk = hs & 1;
      constexpr int nm = H ? 9 : 6;
      constexpr int f0 = hs <= 6 ? (hs * 10) / 7 : hs <= 12 ? 10 + ((hs - 7) * 10) / 6 : 20;
      constexpr int f1 = hs <= 6 ? ((hs + 1) * 10) / 7 : hs <= 12 ? 10 + ((hs - 6) * 10) / 6 : 20;
      sfor<nm>([&](auto N_) {
        constexpr int n = N_.value;
        mm(IC(kk), N_);
        SCHED();
        if constexpr (H) {
          if constexpr (hs < 15 && (!ABL_NOLDS || hs == 0)) rd(IC(hs + 1), N_);
          if constexpr (hs == 7 && n == 0) stat_read(IC(10));
          if constexpr (hs == 11 && n == 2) stat_out(IC(0));
          if constexpr (hs == 12 && (n == 2 || n == 5)) stat_out(IC(n == 2 ? 1 : 2));
        } else {
          if constexpr (hs < 15 && (!ABL_NOLDS || hs == 0)) {
            if constexpr (n < 2) { rd(IC(hs + 1), IC(2 * n)); rd(IC(hs + 1), IC(2 * n + 1)); }
            else rd(IC(hs + 1), IC(n + 2));
          }
          constexpr int B = (hs + 1) / 2;
          if constexpr (!ABL_NODMA) stream(IC(B), IC(hs == 0 || hs == 15 ? n : (n & 1) ? 6 + (1 - kk) * 3 + n / 2 : (1 - kk) * 3 + n / 2));
        }
        SCHED();
      });
      if constexpr (H) {
        sfor<f1 - f0>([&](auto I_) {
          constexpr int f = f0 + I_.value;
          if constexpr (!ABL_NOSTAT) stat_mm(IC(f));
          SCHED();
          if constexpr (f + 2 < 20 && f + 2 != 10 && f + 2 != 11) stat_read(IC(f + 2));
          if constexpr (f == 10) stat_read(IC(11));
          SCHED();
        });
      }
      if constexpr (H && hs == 12) { stat_out(IC(3)); stat_out(IC(4)); SCHED(); }
      if constexpr (hs == 14) {
        const u32 row = 16 + (tid >> 3), ch = c * 8 + (tid & 7);
        xres = *(LAS u32x4*)(L + L_X + (row >> 1) * 2048 + (row & 1) * 512 + (ch >> 5) * 1024 + (((ch & 31) ^ (row & 15)) << 4));
      }
      if constexpr (kk == 0) {
        constexpr int B = s + 1;
        if constexpr (B <= 7) TD(2 * B - 1);
        if constexpr (H) {
          if constexpr (B == 1) wait_vm<0>();
        } else {
          if constexpr (B < 8) wait_vm<(B == 2 || B == 3 ? 11 : 6)>();
        }
        if constexpr (hs == 12 || hs == 14) BARRIER();
        else {
          constexpr int nst = (f0 + 2 < 20 && f0 + 2 != 10 && f0 + 2 != 11 ? 1 : 0) + (f1 - f0 > 1 && f0 + 3 < 20 && f0 + 3 != 10 && f0 + 3 != 11 ? 1 : 0);
          asm volatile("s_waitcnt lgkmcnt(%0)\n\ts_barrier" ::"n"(H ? 2 + nst : 1) : "memory");
        }
        if constexpr (B <= 7) TD(2 * B);
        SCHED();
      }
      if constexpr (!H && hs == 13) {
        const u32 tok = hh * 20 + r16 + (q & 1) * 4;
        const f32x4 p0 = *(LAS f32x4*)(L + L_PART + tok * 32), p1 = *(LAS f32x4*)(L + L_PART + tok * 32 + 16);
        const float m = vadd(vadd(p0.x, p0.z), vadd(p1.x, p1.z)) * (1.0f / D);
        const float v = fmaxf(vadd(vadd(p0.y, p0.w), vadd(p1.y, p1.w)) * (1.0f / D) - m * m, 0.0f);
        *(LAS float*)(L + L_MR + tok * 4) = m;
        *(LAS float*)(L + L_MR + 384 + tok * 4) = __builtin_amdgcn_rsqf(v + EPS);
        SCHED();
      }
    });
    if constexpr (H) {
      const u32 ex = L_X + 65536 + hh * 2048 + lane * 16;
      *(LAS f32x4*)(L + ex) = aK[2];
      *(LAS f32x4*)(L + ex + 1024) = aV[2];
      if (r16 == 0) {
        *(LAS f32x4*)(L + L_CS + hh * 192 + q * 16) = aC;
        *(LAS f32x4*)(L + L_CS + hh * 192 + 64 + q * 16) = aK[0];
      }
      if (q == 0) *(LAS float*)(L + L_CS + hh * 192 + 128 + r16 * 4) = aV[0].x;
    }
  };
  if (heavy) p1_loop(std::true_type{}); else p1_loop(std::false_type{});
  __builtin_amdgcn_s_setprio(0);
  TS(2);
  if (w == 4) {
    const rsrc_t rs_b1 = __builtin_amdgcn_make_buffer_rsrc((void*)b1, 0, F * 2, 0x00020000);
    const rsrc_t rs_b2 = __builtin_amdgcn_make_buffer_rsrc((void*)b2, 0, S * 2, 0x00020000);
    dma4(L_B1, lane * 4, rs_b1, j * 128);
    dma4(L_B2, lane * 4, rs_b2, t * 128);
    __builtin_amdgcn_raw_buffer_store_b64(u32x2{0xffffffffu, 0xffffffffu}, rs_ws, lane * 8, WS_ST + g * 512, 16);
  }
  if (p3) w1_issue(0, stgA);
  BARRIER();
  {
    if (!heavy) {
      const u32 ex = L_X + 65536 + hh * 2048 + lane * 16;
      aK[0] = *(LAS f32x4*)(L + ex);
      aV[0] = *(LAS f32x4*)(L + ex + 1024);
    }
    const f32x4 csQ = *(LAS f32x4*)(L + L_CS + hh * 192 + q * 16), csK = *(LAS f32x4*)(L + L_CS + hh * 192 + 64 + q * 16);
    const float csV = *(LAS float*)(L + L_CS + hh * 192 + 128 + r16 * 4);
    const u32 mr = L_MR + ta * 128;
    float mL[3];
    f32x4 mR[3];
#pragma unroll
    for (int a = 0; a < 3; ++a) {
      mL[a] = *(LAS float*)(L + mr + a * 64 + r16 * 4);
      mR[a] = *(LAS f32x4*)(L + mr + a * 64 + q * 16);
    }
    const u32 k0 = 4 * q + (r16 & 1);
    const bool own0 = k0 <= r16, own1 = k0 + 2 <= r16;
    float rq[2], rk[2][2];
#pragma unroll
    for (int a = 0; a < 2; ++a) {
      rq[a] = *(LAS float*)(L + mr + 384 + (a + 1) * 64 + r16 * 4);
      rk[a][0] = *(LAS float*)(L + mr + 384 + a * 64 + (k0 + (own0 ? 16 : 0)) * 4);
      rk[a][1] = *(LAS float*)(L + mr + 384 + a * 64 + (k0 + 2 + (own1 ? 16 : 0)) * 4);
    }
    u32x2 qf[2], kf[3], vf[3];
#pragma unroll
    for (int a = 0; a < 2; ++a) {
      const f32x4 v = aQ[a] - mL[a + 1] * csQ;
      qf[a] = u32x2{pk(v[0], v[1]), pk(v[2], v[3])};
    }
#pragma unroll
    for (int a = 0; a < 3; ++a) {
      const f32x4 v = aK[a] - mL[a] * csK;
      kf[a] = u32x2{pk(v[0], v[1]), pk(v[2], v[3])};
    }
#pragma unroll
    for (int a = 0; a < 3; ++a) {
      const f32x4 v = aV[a] - mR[a] * csV;
      vf[a] = u32x2{pk(v[0], v[1]), pk(v[2], v[3])};
    }
    const bool odd = r16 & 1;
    float lg[2][2], mx[2];
#pragma unroll
    for (int a = 0; a < 2; ++a) {
      const f32x4 sp = mfma16(kf[a], qf[a], z4);
      const f32x4 so = mfma16(kf[a + 1], qf[a], z4);
      const float cq = rq[a] * (0.25f * 1.44269504088896f);
      const float ev0 = own0 ? so.x : sp.x, od0 = own0 ? so.y : sp.y;
      const float ev1 = own1 ? so.z : sp.z, od1 = own1 ? so.w : sp.w;
      lg[a][0] = (odd ? od0 : ev0) * (rk[a][0] * cq);
      lg[a][1] = (odd ? od1 : ev1) * (rk[a][1] * cq);
      if (a == 0) {
        const bool first = (t == 0) && heavy;
        lg[a][0] = (first && !own0) ? -1.44269504088896e9f : lg[a][0];
        lg[a][1] = (first && !own1) ? -1.44269504088896e9f : lg[a][1];
      }
      mx[a] = vmax(lg[a][0], lg[a][1]);
    }
    allred2<true>(mx[0], mx[1]);
    float ee[2][2], sm[2];
#pragma unroll
    for (int a = 0; a < 2; ++a) {
      ee[a][0] = __builtin_amdgcn_exp2f(lg[a][0] - mx[a]);
      ee[a][1] = __builtin_amdgcn_exp2f(lg[a][1] - mx[a]);
      sm[a] = ee[a][0] + ee[a][1];
    }
    allred2<false>(sm[0], sm[1]);
    u32x2 of[2];
    const u32 sel = odd ? 0x01000c0cu : 0x0c0c0100u, sel1 = odd ? 0x03020c0cu : 0x0c0c0302u;
#pragma unroll
    for (int a = 0; a < 2; ++a) {
      const float inv = __builtin_amdgcn_rcpf(sm[a]);
      const u32 pw = pk(ee[a][0] * rk[a][0] * inv, ee[a][1] * rk[a][1] * inv);
      const u32 p0 = __builtin_amdgcn_perm(0u, pw, sel), p1 = __builtin_amdgcn_perm(0u, pw, sel1);
      const u32x4 pf = {own0 ? 0u : p0, own1 ? 0u : p1, own0 ? p0 : 0u, own1 ? p1 : 0u};
      const u32x4 vv = {vf[a].x, vf[a].y, vf[a + 1].x, vf[a + 1].y};
      const f32x4 o = mfma32(vv, pf, z4);
      of[a] = u32x2{pk(o[0], o[1]), pk(o[2], o[3])};
    }
    auto s0 = __builtin_amdgcn_permlane16_swap(of[0].x, of[1].x, false, false);
    auto s1 = __builtin_amdgcn_permlane16_swap(of[0].y, of[1].y, false, false);
    const u32x4 ov = {s0[0], s1[0], s0[1], s1[1]};
    const u32 ooff = (q & 1) * 2048 + ((q >> 1) * 16 + r16) * 16;
    st_sc1(ov, rs_ws, ooff, WS_O + g * 8192 + ta * 4096 + hh * 512);
  }
  TS(3);
  wait_vm<0>();
  BARRIER();
  if (w == 0 && lane < 8) __builtin_amdgcn_raw_buffer_store_b64(__builtin_bit_cast(u32x2, tag), rs_ws, lane * 128, WS_F1 + t * 1024 + c * 8, 16);
  TS(4);

  u32x4 B1[8][4];
  u32x4 hreg;
  const u32 stg = stgB;
  auto w1_read = [&](int k, u32 lds) {
#pragma unroll
    for (int n = 0; n < 4; ++n) B1[k][n] = trfrag(L, lds + (trN ^ (n << 5)));
  };
  const u32 redw = (q * 16) * 16;
  const u32 s_ = tid >> 3, gq = tid & 7;
  const u32 redr = (((gq >> 1) * 4 + (s_ >> 4)) * 1024) + ((gq & 1) * 32 + ((s_ & 15) ^ (gq << 1))) * 16;
  {
    if (p3) w1_issue(1, stg);
    u32x4 Bo[4][4];
#pragma unroll
    for (int k = 0; k < 4; ++k)
#pragma unroll
      for (int n = 0; n < 4; ++n) Bo[k][n] = trfrag(L, L_RING + hh * 16384 + (trN ^ (n << 5)) + k * 4096);
    if (p3) w1_read(0, L_X + w * 4096);
    BARRIER();
    poll_tag(rs_ws, (lane & 1) * 8, WS_F1 + g * 128 + hh * 16, tag);
    TS(5);
    u32x4 ofr[2][4];
#pragma unroll
    for (int k = 0; k < 4; ++k)
#pragma unroll
      for (int tt = 0; tt < 2; ++tt)
        ofr[tt][k] = ld_sc1(rs_ws, lane * 16 + tt * 2048 + (k & 1) * 1024, WS_O + (t * 8 + 2 * hh + (k >> 1)) * 8192 + th * 4096);
    SCHED();
    f32x4 acc[2][4];
#pragma unroll
    for (int tt = 0; tt < 2; ++tt)
#pragma unroll
      for (int n = 0; n < 4; ++n) acc[tt][n] = f32x4{0, 0, 0, 0};
#pragma unroll
    for (int k = 0; k < 4; ++k)
#pragma unroll
      for (int tt = 0; tt < 2; ++tt)
#pragma unroll
        for (int n = 0; n < 4; ++n) acc[tt][n] = mfma32(Bo[k][n], ofr[tt][k], acc[tt][n]);
    SCHED();
    if (p3) { w1_issue(2, L_X + w * 4096); }
#pragma unroll
    for (int n = 0; n < 4; ++n)
#pragma unroll
      for (int tt = 0; tt < 2; ++tt)
        *(LAS f32x4*)(L + L_RING + hh * 16384 + (n * 4 + th * 2 + tt) * 1024 + redw + ((r16 ^ ((2 * n + (q >> 1)) << 1)) << 4)) = acc[tt][n];
    BARRIER();
    TS(6);
    f32x4 a0 = *(LAS f32x4*)(L + L_RING + redr), a1 = *(LAS f32x4*)(L + L_RING + redr + 256);
#pragma unroll
    for (int k = 1; k < 4; ++k) {
      a0 += *(LAS f32x4*)(L + L_RING + k * 16384 + redr);
      a1 += *(LAS f32x4*)(L + L_RING + k * 16384 + redr + 256);
    }
    hreg = u32x4{pk(blo(xres.x) + a0[0], bhi(xres.x) + a0[1]), pk(blo(xres.y) + a0[2], bhi(xres.y) + a0[3]),
                 pk(blo(xres.z) + a1[0], bhi(xres.z) + a1[1]), pk(blo(xres.w) + a1[2], bhi(xres.w) + a1[3])};
    const f32x4 h0 = {blo(hreg.x), bhi(hreg.x), blo(hreg.y), bhi(hreg.y)}, h1 = {blo(hreg.z), bhi(hreg.z), blo(hreg.w), bhi(hreg.w)};
    const f32x4 hs = h0 + h1, hq = h0 * h0 + h1 * h1;
    const float ps = sum8((hs.x + hs.y) + (hs.z + hs.w)), pq = sum8((hq.x + hq.y) + (hq.z + hq.w));
    if (gq == 0) __builtin_amdgcn_raw_buffer_store_b64(u32x2{min(f2u(ps), 0xfffffffeu), min(f2u(pq), 0xfffffffeu)}, rs_ws, s_ * 8, WS_ST + g * 512, 16);
    TS(7);
    if (p3) {
      w1_read(1, stg);
      SCHED();
      WAIT_LGKM0();
      SCHED();
      w1_issue(3, stg);
    }
    u32x2 sv;
    for (int sp = 0; sp < SPIN_CAP; ++sp) {
      FENCE();
      sv = __builtin_amdgcn_raw_buffer_load_b64(rs_ws, gq * 512 + s_ * 8, WS_ST + t * 4096, 16);
      if (!__any(max(sv.x, sv.y) == 0xffffffffu)) break;
    }
    TS(8);
    {
      const float m = sum8(u2f(sv.x)) * (1.0f / D);
      const float v = fmaxf(sum8(u2f(sv.y)) * (1.0f / D) - m * m, 0.0f);
      const float r1 = __builtin_amdgcn_rsqf(v + EPS);
      const float r2 = __builtin_amdgcn_rsqf(v * r1 * r1 + EPS);
      const float r = r1 * r2;
      const f32x4 n0 = (h0 - m) * r, n1 = (h1 - m) * r;
      const u32x4 hn = {pk(n0[0], n0[1]), pk(n0[2], n0[3]), pk(n1[0], n1[1]), pk(n1[2], n1[3])};
      const u32 ta = L_RING + 65536 + w * 1024;
      *(LAS u32x4*)(L + ta + ((lane * 16) ^ (((lane >> 4) & 1) << 6))) = hn;
      FENCE();
      const u32x4 hf = trfrag(L, ta + qp * 128 + ((q * 32) ^ ((qp >> 1) << 6)) + pp * 8);
      st_sc1(hf, rs_ws, q * 1024 + r16 * 16, WS_HN + c * 262144 + (2 * t + th) * 4096 + hh * 256);
    }
    wait_vm<0>();
    BARRIER();
    if (w == 0 && lane < 32) __builtin_amdgcn_raw_buffer_store_b64(__builtin_bit_cast(u32x2, tag), rs_ws, lane * 2048, WS_F2 + c * 256 + t * 8, 16);
    TS(9);
  }

  const u32 w2E = (lane >> 3) * (S * 2) + swzE, w2O = (lane >> 3) * (S * 2) + (swzE ^ 64) + 8 * (S * 2) - 1024;
  const u32 w2s = w * 224 * (S * 2) + t * 128;
  if (p3) {
    w1_issue(4, L_RING + w * 8192);
    w1_issue(5, L_RING + w * 8192 + 4096);
    w1_read(2, L_X + w * 4096);
    w1_read(3, stg);
    SCHED();
    WAIT_LGKM0();
    SCHED();
    w1_issue(6, L_X + w * 4096);
    w1_issue(7, stg);
    wait_vm<12>();
    w1_read(4, L_RING + w * 8192);
    wait_vm<8>();
    w1_read(5, L_RING + w * 8192 + 4096);
    wait_vm<4>();
    w1_read(6, L_X + w * 4096);
    wait_vm<0>();
    w1_read(7, stg);
    const u32 mte = w >> 1, kp = w & 1;
    const u32x4 bb = *(LAS u32x4*)(L + L_B1 + kp * 64 + q * 16);
    BARRIER();
    poll_tag(rs_ws, (lane & 3) * 8, WS_F2 + g * 256 + w * 32, tag);
    TS(10);
    constexpr int RING = RING3;
    u32x4 A[RING];
    const u32 as = WS_HN + c * 262144 + w * 32768;
    u32 ak[8];
#pragma unroll
    for (int k = 0; k < 8; ++k) ak[k] = as + ((k + rot1) & 7) * 4096;
#pragma unroll
    for (int i = 0; i < RING; ++i) A[i] = ld_sc1(rs_ws, lane * 16 + (i >> 3) * 1024, ak[i & 7]);
    SCHED();
#pragma unroll
    for (int mt = 0; mt < 4; ++mt) {
      f32x4 acc[4];
#pragma unroll
      for (int n = 0; n < 4; ++n) acc[n] = f32x4{0, 0, 0, 0};
#pragma unroll
      for (int k = 0; k < 8; ++k) {
        const int i = mt * 8 + k;
        const u32x4 cur = A[i % RING];
#pragma unroll
        for (int n = 0; n < 4; ++n) acc[n] = mfma32(B1[k][n], cur, acc[n]);
        if (i + RING < 32) A[i % RING] = ld_sc1(rs_ws, lane * 16 + ((i + RING) >> 3) * 1024, ak[(i + RING) & 7]);
        SCHED();
      }
#pragma unroll
      for (int n = 0; n < 4; ++n) *(LAS f32x4*)(L + ((w * 4 + mt) * 4 + n) * 1024 + lane * 16) = acc[n];
    }
    BARRIER();
    TS(11);
    if (g == 0 && tid == 0) *(GAS u64*)(ws + WS_EPOCH) = epoch + 1;
    {
      f32x4 s0 = {blo(bb.x), bhi(bb.x), blo(bb.y), bhi(bb.y)}, s1 = {blo(bb.z), bhi(bb.z), blo(bb.w), bhi(bb.w)};
      const u32 ra = (mte * 4 + 2 * kp + (q >> 1)) * 1024 + ((q & 1) * 32 + r16) * 16;
#pragma unroll
      for (int ww = 0; ww < 8; ++ww) {
        s0 += *(LAS f32x4*)(L + ww * 16384 + ra);
        s1 += *(LAS f32x4*)(L + ww * 16384 + ra + 256);
      }
      float v[8] = {s0[0], s0[1], s0[2], s0[3], s1[0], s1[1], s1[2], s1[3]};
#pragma unroll
      for (int e = 0; e < 8; ++e) {
        const float z = v[e] * (v[e] * v[e] * (-0.044715f * 2.302208198f) - 2.302208198f);
        v[e] = v[e] * __builtin_amdgcn_rcpf(1.0f + __builtin_amdgcn_exp2f(z));
      }
      const u32x4 uf = {pk(v[0], v[1]), pk(v[2], v[3]), pk(v[4], v[5]), pk(v[6], v[7])};
      st_sc1(uf, rs_ws, lane * 16, WS_U + c * (56 * 4096) + (2 * j + kp) * 4096 + mte * 1024);
    }
    wait_vm<0>();
    BARRIER();
    if (w == 0 && lane < 32) __builtin_amdgcn_raw_buffer_store_b64(__builtin_bit_cast(u32x2, tag), rs_ws, lane * 2048, WS_F3 + c * 256 + j * 8, 16);
  } else {
    const u32 tv = (lane >> 3) * (S * 2) + (lane & 7) * 128;
#pragma unroll
    for (int i = 0; i < 7; ++i) dma4(L_SCR, tv, rs_w2, (((c & 3) * 8 + w) * 7 + i) * 8 * (S * 2) + sq * 1024);
    wait_vm<0>();
  }
  TS(12);

  {
    u32x4 B2[7][4];
    auto w2_issue = [&](int k, u32 lds) { dma16x4<8 * S * 2>(lds, w2E, w2O, rs_w2, w2s + k * 32 * (S * 2)); };
    auto w2_read = [&](int k, u32 lds) {
#pragma unroll
      for (int n = 0; n < 4; ++n) B2[k][n] = trfrag(L, lds + (trN ^ (n << 5)));
    };
#pragma unroll
    for (int k = 0; k < 4; ++k) w2_issue(k, w * 16384 + k * 4096);
#pragma unroll
    for (int k = 0; k < 4; ++k) {
      wait_vm<12>();
      w2_read(k, w * 16384 + k * 4096);
      if (k < 3) {
        SCHED();
        WAIT_LGKM0();
        SCHED();
        w2_issue(4 + k, w * 16384 + k * 4096);
      }
    }
    wait_vm<8>();
    w2_read(4, w * 16384);
    wait_vm<4>();
    w2_read(5, w * 16384 + 4096);
    wait_vm<0>();
    w2_read(6, w * 16384 + 8192);
    const float bias = blo((u32) * (LAS u16*)(L + L_B2 + s_ * 2));
    BARRIER();
    TS(13);
    poll_tag(rs_ws, (lane & 3) * 8, WS_F3 + g * 256 + ((7 * w) >> 1) * 8, tag);
    TS(14);
    constexpr int RING = RING4;
    u32x4 A[RING];
    const u32 as = WS_U + c * (56 * 4096) + w * (7 * 4096);
#pragma unroll
    for (int i = 0; i < RING; ++i) A[i] = ld_sc1(rs_ws, lane * 16 + (i / 7) * 1024, as + (i % 7) * 4096);
    SCHED();
#pragma unroll
    for (int mt = 0; mt < 4; ++mt) {
      f32x4 acc[4];
#pragma unroll
      for (int n = 0; n < 4; ++n) acc[n] = f32x4{0, 0, 0, 0};
#pragma unroll
      for (int k = 0; k < 7; ++k) {
        const int i = mt * 7 + k;
        const u32x4 cur = A[i % RING];
#pragma unroll
        for (int n = 0; n < 4; ++n) acc[n] = mfma32(cur, B2[k][n], acc[n]);
        if (i + RING < 28) A[i % RING] = ld_sc1(rs_ws, lane * 16 + ((i + RING) / 7) * 1024, as + ((i + RING) % 7) * 4096);
        SCHED();
      }
      const u32 wl = redw + ((r16 ^ ((2 * mt + (q >> 1)) << 1)) << 4);
#pragma unroll
      for (int n = 0; n < 4; ++n) *(LAS f32x4*)(L + ((w * 4 + mt) * 4 + n) * 1024 + wl) = acc[n];
    }
    BARRIER();
    {
      f32x4 s0 = {blo(hreg.x) + bias, bhi(hreg.x) + bias, blo(hreg.y) + bias, bhi(hreg.y) + bias};
      f32x4 s1 = {blo(hreg.z) + bias, bhi(hreg.z) + bias, blo(hreg.w) + bias, bhi(hreg.w) + bias};
#pragma unroll
      for (int ww = 0; ww < 8; ++ww) {
        s0 += *(LAS f32x4*)(L + ww * 16384 + redr);
        s1 += *(LAS f32x4*)(L + ww * 16384 + redr + 256);
      }
      const u32x4 ov = {pk(s0[0], s0[1]), pk(s0[2], s0[3]), pk(s1[0], s1[1]), pk(s1[2], s1[3])};
      __builtin_amdgcn_raw_buffer_store_b128(ov, __builtin_amdgcn_make_buffer_rsrc((void*)out, 0, S * D * 2, 0x00020000), s_ * 1024 + gq * 16, t * 65536 + c * 128, OUT_AUX);
    }
  }
  while (__builtin_amdgcn_s_memrealtime() - fix0 < FIXT) __builtin_amdgcn_s_sleep(1);
  asm volatile(".Lfb_end_mark:" ::: "memory");
}

extern "C" void kernel_launch(void* const* d_in, const int* in_sizes, int n_in, void* d_out, int out_size, void* d_ws, size_t ws_size,
                              hipStream_t stream) {
  (void)in_sizes; (void)n_in; (void)out_size; (void)ws_size;
  (void)hipFuncSetAttribute((const void*)fused_block, hipFuncAttributeMaxDynamicSharedMemorySize, L_TOTAL);
  fused_block<<<256, 512, L_TOTAL, stream>>>((const u16*)d_in[0], (const u16*)d_in[1], (char*)d_ws, (const u16*)d_in[2], (const u16*)d_in[4],
                                             (const u16*)d_in[6], (const u16*)d_in[5], (const u16*)d_in[7], (u16*)d_out);
}
```

```cpp
#include <hip/hip_runtime.h>
#include <utility>
#pragma clang diagnostic ignored "-Wint-to-pointer-cast"

typedef unsigned short u16;
typedef unsigned u32;
typedef unsigned long long u64;
typedef __bf16 bf16x8 __attribute__((ext_vector_type(8)));
typedef __bf16 bf16x2 __attribute__((ext_vector_type(2)));
typedef short s16x4 __attribute__((ext_vector_type(4)));
typedef float f32x4 __attribute__((ext_vector_type(4)));
typedef float f32x2 __attribute__((ext_vector_type(2)));
typedef u32 u32x4 __attribute__((ext_vector_type(4)));
typedef u32 u32x2 __attribute__((ext_vector_type(2)));
#define LAS __attribute__((address_space(3)))
#define GAS __attribute__((address_space(1)))
#define DI __device__ __forceinline__
typedef __amdgpu_buffer_rsrc_t rsrc_t;

#define STAMP_TID 0
#define SDELAY 0
#define S3N 2
#define RING3 7
#define RING4 8
#define NPRE2 2

constexpr int S = 2048, D = 512, F = 1792;
constexpr float EPS = 1e-5f;
constexpr u32 WS_EPOCH = 0;
constexpr u32 WS_F1 = 4096;
constexpr u32 WS_F2 = WS_F1 + 256 * 128;
constexpr u32 WS_F3 = WS_F2 + 256 * 256;
constexpr u32 WS_F4 = WS_F3 + 256 * 256;
constexpr u32 WS_ST = WS_F4 + 256 * 256;
constexpr u32 WS_O = 2u << 20;
constexpr u32 WS_HN = WS_O + (2u << 20);
constexpr u32 WS_U = WS_HN + (2u << 20);
constexpr u32 WS_END = WS_U + (2u << 20);
constexpr u32 WS_STAMP = WS_END;
constexpr u32 L_RING = 0;
constexpr u32 L_X = 81920;
constexpr u32 L_MISC = 73728;
constexpr u32 L_PART = L_MISC;
constexpr u32 L_CS = L_MISC + 4096;
constexpr u32 L_MR = L_MISC + 5120;
constexpr u32 L_SCR = L_MISC + 6144;
constexpr u32 L_B1 = L_MISC + 7680;
constexpr u32 L_B2 = L_MISC + 7936;
constexpr u32 L_TOTAL = 163840;
constexpr int SPIN_CAP = 1 << 18;

DI void dma4(u32 lds, u32 voff, rsrc_t rs, u32 soff) {
  asm volatile("s_mov_b32 m0, %0\n\ts_nop 0\n\tbuffer_load_dword %1, %2, %3 offen lds" ::"s"(lds), "v"(voff), "s"(rs), "s"(soff) : "memory");
}
template <int LC, int SC>
DI void dma16c(u32 lds, u32 voff, rsrc_t rs, u32 soff) {
  u32 tmp;
  asm volatile("s_add_u32 m0, %1, %5\n\ts_add_u32 %0, %4, %6\n\tbuffer_load_dwordx4 %2, %3, %0 offen lds"
               : "=&s"(tmp) : "s"(lds), "v"(voff), "s"(rs), "s"(soff), "n"(LC), "n"(SC) : "memory", "scc");
}
template <int LC, int SC>
DI void dma16m(u32 lds, u32 voff, rsrc_t rs, u32 soff, u64 mask) {
  u32 tmp;
  asm volatile("s_add_u32 m0, %1, %5\n\ts_add_u32 %0, %4, %6\n\ts_mov_b64 exec, %7\n\tbuffer_load_dwordx4 %2, %3, %0 offen lds\n\ts_mov_b64 exec, -1"
               : "=&s"(tmp) : "s"(lds), "v"(voff), "s"(rs), "s"(soff), "n"(LC), "n"(SC), "s"(mask) : "memory", "scc");
}
template <int STEP, int LC>
DI void dma16x4(u32 lds, u32 voffE, u32 voffO, rsrc_t rs, u32 soff) {
  u32 tmp;
  asm volatile(
      "s_add_u32 m0, %1, %7\n\t"
      "s_add_u32 %0, %5, %6\n\t"
      "buffer_load_dwordx4 %2, %4, %5 offen lds\n\t"
      "buffer_load_dwordx4 %3, %4, %5 offen offset:1024 lds\n\t"
      "buffer_load_dwordx4 %2, %4, %0 offen offset:2048 lds\n\t"
      "buffer_load_dwordx4 %3, %4, %0 offen offset:3072 lds"
      : "=&s"(tmp)
      : "s"(lds), "v"(voffE), "v"(voffO), "s"(rs), "s"(soff), "n"(2 * STEP - 2048), "n"(LC)
      : "memory", "scc");
}
template <int N> DI void wait_vm() { asm volatile("s_waitcnt vmcnt(%0)" ::"n"(N) : "memory"); }
template <int N> DI void wait_tie(u32x4& v) { asm volatile("s_waitcnt vmcnt(%1)" : "+v"(v) : "n"(N) : "memory"); }
template <int N> DI void barrier_lgkm() { asm volatile("s_waitcnt lgkmcnt(%0)\n\ts_barrier" ::"n"(N) : "memory"); }
#define WAIT_LGKM0() asm volatile("s_waitcnt lgkmcnt(0)" ::: "memory")
#define BARRIER() barrier_lgkm<0>()
#define SCHED() __builtin_amdgcn_sched_barrier(0)
#define FENCE() asm volatile("" ::: "memory")
#define PINV(x) asm volatile("" : "+v"(x))
#define PINS(x) asm volatile("" : "+s"(x))
#define IC(n) std::integral_constant<int, (n)>{}
#define LD(T, a) (*(LAS T*)(a))

DI f32x4 mfma32(u32x4 a, u32x4 b, f32x4 c) {
  return __builtin_amdgcn_mfma_f32_16x16x32_bf16(__builtin_bit_cast(bf16x8, a), __builtin_bit_cast(bf16x8, b), c, 0, 0, 0);
}
DI f32x4 mfma16(u32x2 a, u32x2 b, f32x4 c) {
  return __builtin_amdgcn_mfma_f32_16x16x16bf16_1k(__builtin_bit_cast(s16x4, a), __builtin_bit_cast(s16x4, b), c, 0, 0, 0);
}
DI u32x2 trd(u32 addr) { return __builtin_bit_cast(u32x2, __builtin_amdgcn_ds_read_tr16_b64_v4i16((LAS s16x4*)(addr))); }
DI u32x4 trfrag(u32 a) {
  u32x2 lo = trd(a), hi = trd(a + 512);
  return u32x4{lo.x, lo.y, hi.x, hi.y};
}
DI u32 pk(float a, float b) {
  f32x2 v = {a, b};
  return __builtin_bit_cast(u32, __builtin_convertvector(v, bf16x2));
}
DI float blo(u32 v) { return __builtin_bit_cast(float, v << 16); }
DI float bhi(u32 v) { return __builtin_bit_cast(float, v & 0xffff0000u); }
DI u32 f2u(float f) { return __builtin_bit_cast(u32, f); }
DI float u2f(u32 u) { return __builtin_bit_cast(float, u); }
DI float vmax(float a, float b) { return __builtin_elementwise_maximum(a, b); }
DI float mul1(float a, float b) { float r = a * b; PINV(r); return r; }
template <bool MAX>
DI void allred2(float& a, float& b) {
  auto r1 = __builtin_amdgcn_permlane16_swap(f2u(a), f2u(b), false, false);
  float t = MAX ? vmax(u2f(r1[0]), u2f(r1[1])) : u2f(r1[0]) + u2f(r1[1]);
  auto r2 = __builtin_amdgcn_permlane32_swap(f2u(t), f2u(t), false, false);
  float u = MAX ? vmax(u2f(r2[0]), u2f(r2[1])) : u2f(r2[0]) + u2f(r2[1]);
  auto r3 = __builtin_amdgcn_permlane16_swap(f2u(u), f2u(u), false, false);
  a = u2f(r3[0]);
  b = u2f(r3[1]);
}
template <int CTRL> DI float dppadd(float v) { return v + __builtin_amdgcn_update_dpp(0.0f, v, CTRL, 0xf, 0xf, true); }
DI float sum8(float v) {
  v = dppadd<0xB1>(v);
  v = dppadd<0x4E>(v);
  v = dppadd<0x141>(v);
  return v;
}
DI u32x4 ld_sc1(rsrc_t rs, u32 voff, u32 soff) { return __builtin_amdgcn_raw_buffer_load_b128(rs, voff, soff, 16); }
DI void st_sc1(u32x4 v, rsrc_t rs, u32 voff, u32 soff) { __builtin_amdgcn_raw_buffer_store_b128(v, rs, voff, soff, 16); }
DI void st8_sc1(u32x2 v, rsrc_t rs, u32 voff, u32 soff) { __builtin_amdgcn_raw_buffer_store_b64(v, rs, voff, soff, 16); }

DI void put8(char* base, u32 off, u64 v) { (void)__hip_atomic_exchange((GAS u64*)(base + off), v, __ATOMIC_RELAXED, __HIP_MEMORY_SCOPE_AGENT); }
DI u64 get8(char* base, u32 off) {
  u64 zero = 0;
  PINV(zero);
  return __hip_atomic_fetch_add((GAS u64*)(base + off), zero, __ATOMIC_RELAXED, __HIP_MEMORY_SCOPE_AGENT);
}
template <int N>
DI void poll_tag(char* base, u32 lane, u64 tag) {
  if (lane < N) {
    for (int sp = 0; sp < SPIN_CAP; ++sp)
      if (__all(get8(base, lane * 8) == tag)) break;
  }
  FENCE();
}
template <typename Fn, int... I> DI void sfor_impl(Fn&& f, std::integer_sequence<int, I...>) { (f(std::integral_constant<int, I>{}), ...); }
template <int N, typename Fn> DI void sfor(Fn&& f) { sfor_impl(f, std::make_integer_sequence<int, N>{}); }

#define TS(i)
#define TD(i)

constexpr int stat_f0(int hs) {
  constexpr int v[14] = {0, 1, 2, 4, 5, 6, 8, 9, 10, 12, 13, 14, 15, 16};
  return v[hs < 13 ? hs : 13];
}

__global__ __launch_bounds__(512, 2) void fused_block(const u16* __restrict__ x, const u16* __restrict__ wqkv, char* __restrict__ ws,
                                                      const u16* __restrict__ wo, const u16* __restrict__ w1, const u16* __restrict__ w2,
                                                      const u16* __restrict__ b1, const u16* __restrict__ b2, u16* __restrict__ out) {
  TS(0);
  const u32 tid = threadIdx.x, lane = tid & 63;
  const u32 w = __builtin_amdgcn_readfirstlane(tid >> 6);
  const u32 b = blockIdx.x, xcd = b & 7, idx = b >> 3;
  const u32 c = (xcd & 1) * 4 + (idx & 3), tl = idx >> 2, sq = xcd >> 1, t = sq * 8 + tl;
  const u32 c1 = c ^ 1;
  const u32 hh = w & 3, th = w >> 2;
  const bool heavy = w >= 4;
  const u32 r16 = lane & 15, q = lane >> 4;

  const rsrc_t rs_x = __builtin_amdgcn_make_buffer_rsrc((void*)x, 0, S * D * 2, 0x00020000);
  const rsrc_t rs_wqkv = __builtin_amdgcn_make_buffer_rsrc((void*)wqkv, 0, D * 3 * D * 2, 0x00020000);

  const u32 xrw = 4 * hh + q;
  const u32 xchunk = (r16 ^ xrw) << 4;
  const u32 xv = xrw * 1024 + xchunk;
  const u32 xv0 = t == 0 ? xchunk : xv;
  const u32 xs0 = t == 0 ? 0 : t * 65536 - 16384;
  const u64 xmask = hh == 0 ? ~0xffffull : ~0ull;
  const u32 swzE = ((lane & 7) ^ ((q & 1) << 1)) << 4;
  const u32 swzW = swzE ^ ((hh & 1) << 6);
  const u32 wv = (hh * 8 + (lane >> 3)) * 3072 + swzW;
  auto dma_x = [&](auto A_, auto Q_) {
    constexpr int a = A_.value, qt = Q_.value;
    if constexpr (a == 0) dma16m<L_X + qt * 1024, qt * 256>(hh * 4096, xv0, rs_x, xs0, xmask);
    else dma16c<L_X + a * 16384 + qt * 1024, (a - 1) * 16384 + qt * 256>(hh * 4096, xv, rs_x, t * 65536);
  };
  auto dma_w = [&](auto S_, auto I_) {
    constexpr int s = S_.value, seg = I_.value % 3, hi = I_.value / 3;
    dma16c<L_RING + ((s + 1) % 3) * 24576 + hi * 12288 + seg * 4096, (s * 64 + hi * 32) * 3072 + seg * 1024>(hh * 1024, wv, rs_wqkv, c1 * 128);
  };
  asm volatile("s_nop 4" ::: "memory");
  if (heavy) {
    sfor<6>([&](auto i) { dma_w(IC(0), i); });
    sfor<6>([&](auto i) { dma_w(IC(1), i); });
  } else {
    sfor<5>([&](auto a) { dma_x(a, IC(0)); });
    sfor<5>([&](auto a) { dma_x(a, IC(1)); });
  }
  const u32 g = t * 8 + c;
  const u32 tidle = (3u - ((u32)(size_t)w2 >> 7)) & 7u;
  const bool p3 = tl != tidle;
  const u32 j = sq * 7 + (tl > tidle ? tl - 1 : p3 ? tl : 0);
  const rsrc_t rs_wo = __builtin_amdgcn_make_buffer_rsrc((void*)wo, 0, D * D * 2, 0x00020000);
  const rsrc_t rs_w1 = __builtin_amdgcn_make_buffer_rsrc((void*)w1, 0, p3 ? S * F * 2 : 0, 0x00020000);
  const rsrc_t rs_w2 = __builtin_amdgcn_make_buffer_rsrc((void*)w2, 0, F * S * 2, 0x00020000);
  const rsrc_t rs_ws = __builtin_amdgcn_make_buffer_rsrc((void*)ws, 0, WS_END, 0x00020000);
  const rsrc_t rs_out = __builtin_amdgcn_make_buffer_rsrc((void*)out, 0, S * D * 2, 0x00020000);
  if (w == 7) {
    u64 pc;
    u32 nbytes;
    asm volatile("s_getpc_b64 %0\n.Lfb_pc_mark:\n\ts_mov_b32 %1, .Lfb_end_mark-.Lfb_pc_mark" : "=s"(pc), "=s"(nbytes));
    const rsrc_t rs_pc = __builtin_amdgcn_make_buffer_rsrc((void*)(pc & ~(u64)15), 0, nbytes, 0x00020000);
    dma16c<L_SCR, 0>(0, lane * 16, rs_pc, idx * 1024);
  }
  if (w == 5) {
    const u32 cs = (3u - ((u32)(size_t)wqkv >> 7)) & 7u;
    if ((cs >> 2) == (c1 >> 2) && cs != c1) dma4(L_SCR, lane * 3072, rs_wqkv, tl * 64 * 3072 + ((c1 - cs - 1) & 3) * 1024 + cs * 128);
  }
  if (w == 6) {
    const rsrc_t rs_b1 = __builtin_amdgcn_make_buffer_rsrc((void*)b1, 0, F * 2, 0x00020000);
    const rsrc_t rs_b2 = __builtin_amdgcn_make_buffer_rsrc((void*)b2, 0, S * 2, 0x00020000);
    dma4(L_B1, lane * 4, rs_b1, j * 128);
    dma4(L_B2, lane * 4, rs_b2, t * 128);
    st8_sc1(u32x2{~0u, ~0u}, rs_ws, lane * 8, WS_ST + g * 512);
  }
  u32x4 ones = {0x3f803f80u, 0x3f803f80u, 0x3f803f80u, 0x3f803f80u};
  PINV(ones);
  if (w == 5) LD(u32x4, L_X + q * 1024 + r16 * 16) = ones;
  u64 epoch;
  asm volatile("s_load_dwordx2 %0, %1, 0x0\n\ts_waitcnt lgkmcnt(0)" : "=s"(epoch) : "s"(ws));
  u64 tag = (epoch + 1) * 0x9E3779B97F4A7C15ull + 0x632BE59BD9B4E019ull;
  PINS(tag);

  const u32 ta = heavy ? 0u : 1u;
  const u32 qp = r16 >> 2, pp = lane & 3;
  u32 trN = (8 * q + qp) * 128 + ((((qp >> 1) & 1) | ((q & 1) << 1)) << 5) + 8 * pp;
  u32 w1E = (lane >> 3) * (F * 2) + swzE, w1O = (lane >> 3) * (F * 2) + (swzE ^ 64) + 8 * (F * 2) - 1024;
  const u32 k0 = 4 * q + (r16 & 1);
  const bool own0 = k0 <= r16, own1 = k0 + 2 <= r16, odd = r16 & 1;
  u32 mrL = L_MR + ta * 128 + r16 * 4, mrR = L_MR + ta * 128 + q * 16;
  u32 rk0a = L_MR + 384 + ta * 128 + (k0 + (own0 ? 16 : 0)) * 4, rk1a = L_MR + 384 + ta * 128 + (k0 + 2 + (own1 ? 16 : 0)) * 4;
  u32 sel0 = odd ? 0x01000c0cu : 0x0c0c0100u, sel1 = odd ? 0x03020c0cu : 0x0c0c0302u;
  u32 csa = L_CS + hh * 192 + q * 16, csv = L_CS + hh * 192 + 128 + r16 * 4;
  u32 exa = L_X + hh * 2048 + lane * 16;
  u32 ooff = (q & 1) * 2048 + ((q >> 1) * 16 + r16) * 16;
  const u32 s_ = tid >> 3, gq = tid & 7;
  u32 xra;
  {
    const u32 row = 16 + s_, ch = c * 8 + gq;
    xra = L_X + (row >> 2) * 4096 + (ch >> 4) * 1024 + (row & 3) * 256 + (((ch & 15) ^ (row & 15)) << 4);
  }
  PINV(trN); PINV(w1E); PINV(w1O); PINV(mrL); PINV(mrR); PINV(rk0a); PINV(rk1a); PINV(sel0); PINV(sel1); PINV(csa); PINV(csv); PINV(exa);
  PINV(ooff); PINV(xra);
  u32 trO[4];
#pragma unroll
  for (int n = 0; n < 4; ++n) { trO[n] = (trN ^ (n << 5)) + hh * 16384; PINV(trO[n]); }
  constexpr u32 rot1 = 0;
  u32 w1s = w * 256 * (F * 2) + j * 128, stg = w * 4096;
  PINS(w1s); PINS(stg);
  auto w1_issue = [&](auto K_, auto LC_) {
    dma16x4<8 * F * 2, LC_.value>(stg, w1E, w1O, rs_w1, w1s + ((K_.value + rot1) & 7) * 32 * (F * 2));
  };
  auto w1_piece = [&](auto K_, auto P_, auto LC_) {
    constexpr int p = P_.value;
    SCHED();
    dma16c<LC_.value + p * 1024, (K_.value * 32 + (p >> 1) * 16) * (F * 2) + (p & 1) * 1024>(stg, (p & 1) ? w1O : w1E, rs_w1, w1s);
    SCHED();
  };
  constexpr u32 SC = 65536, SA = 98304, SB = 131072;

  f32x4 aQ[2], aK[3], aV[3];
  const f32x4 z4 = {0, 0, 0, 0};
  u32x4 xres;
  float mL[3], fq[2][2], rk[2][2];
  f32x4 mR[3];

  auto p1_loop = [&](auto H_) {
    constexpr bool H = H_.value;
    u32x4 Xf[2][3], Wf[2][3], Sf[2];
    f32x4 sA = z4, aC = z4;
#pragma unroll
    for (int i = 0; i < 2; ++i) aQ[i] = z4;
#pragma unroll
    for (int i = 0; i < 3; ++i) { aK[i] = z4; aV[i] = z4; }
    const u32 xl = L_X + (r16 >> 2) * 4096 + (r16 & 3) * 256 + ((q ^ r16) << 4);
    u32 xb[4], xst[4], wr = trN ^ (hh << 5), wr2 = wr + 32768;
#pragma unroll
    for (int i = 0; i < 4; ++i) { xb[i] = (xl ^ (i << 6)) + (H ? 0 : 32768); PINV(xb[i]); }
    PINV(wr); PINV(wr2);
    u32 wov = (hh * 8 + (lane >> 3)) * 1024 + swzW;
    const u32 stok = min(10 * w + 3 * qp + pp, 79u), srow = pp == 3 ? 0u : stok;
    u32 fin = L_MR + stok * 4;
#pragma unroll
    for (int i = 0; i < 4; ++i) {
      xst[i] = L_X + (srow >> 2) * 4096 + (srow & 3) * 256 + (((q + 4 * i) ^ (srow & 15)) << 4);
      PINV(xst[i]);
    }
    PINV(fin);
    if constexpr (!H) PINV(wov);
    u32x2 wh;
    float snm, srv;
    auto rd = [&](auto HS_, auto N_) {
      constexpr int hs = HS_.value, s = hs / 2, kk = hs & 1, n = N_.value;
      constexpr u32 so = L_RING + ((s + 1) % 3) * 24576 + kk * 12288, xo = (hs >> 2) * 1024;
      if constexpr (n == 0) Xf[kk][1] = LD(u32x4, xb[hs & 3] + xo + 16384);
      if constexpr (n >= 1 && n <= 6) {
        constexpr int seg = (n - 1) / 2;
        constexpr u32 o = so + seg * 4096 + ((n - 1) & 1) * 512;
        const u32x2 v = o < 65536 ? trd(wr + o) : trd(wr2 + (o - 32768));
        if constexpr (n & 1) wh = v;
        else Wf[kk][seg] = u32x4{wh.x, wh.y, v.x, v.y};
      }
      if constexpr (n == 7) Xf[kk][2] = LD(u32x4, xb[hs & 3] + xo + 32768);
      if constexpr (n == 8) Xf[kk][0] = LD(u32x4, xb[hs & 3] + xo);
    };
    auto mm = [&](auto KK_, auto N_) {
      constexpr int kk = KK_.value, n = N_.value;
      if constexpr (n == 0) aQ[0] = mfma32(Wf[kk][0], Xf[kk][1], aQ[0]);
      if constexpr (n == 1) aK[1] = mfma32(Wf[kk][1], Xf[kk][1], aK[1]);
      if constexpr (n == 2) aV[1] = mfma32(Xf[kk][1], Wf[kk][2], aV[1]);
      if constexpr (n == 3) aQ[1] = mfma32(Wf[kk][0], Xf[kk][2], aQ[1]);
      if constexpr (n == 4) aK[2] = mfma32(Wf[kk][1], Xf[kk][2], aK[2]);
      if constexpr (n == 5) aV[2] = mfma32(Xf[kk][2], Wf[kk][2], aV[2]);
      if constexpr (H && n == 6) aK[0] = mfma32(Wf[kk][1], Xf[kk][0], aK[0]);
      if constexpr (H && n == 7) aV[0] = mfma32(Xf[kk][0], Wf[kk][2], aV[0]);
      if constexpr (!H && n == 6) aC = mfma32(Wf[kk][0], ones, aC);
    };
    auto stream = [&](auto B_, auto I_) {
      constexpr int B = B_.value, i = I_.value;
      if constexpr (B <= 5 && i < 6) dma_w(IC(B + 2), I_);
      else if constexpr (B <= 3 && i < ((B & 1) ? 8 : 9)) dma_x(IC(i - 6 + (B & 1) * 3), IC(2 + B / 2));
      else if constexpr (B >= 6 && i < (B == 8 ? 4 : 6)) {
        constexpr int p = (B - 6) * 6 + i;
        dma16c<L_RING + p * 4096, p * 32768>(hh * 1024, wov, rs_wo, c * 128);
      }
    };
    wait_vm<(H ? 6 : 5)>();
    BARRIER();
    TS(1);
    sfor<(H ? 9 : 8)>([&](auto n) { rd(IC(0), n); });
    SCHED();
    sfor<16>([&](auto HS_) {
      constexpr int hs = HS_.value, s = hs / 2, kk = hs & 1;
      constexpr int f0 = stat_f0(hs), f1 = stat_f0(hs + 1);
      sfor<(H ? 8 : 7)>([&](auto N_) {
        constexpr int n = N_.value;
        mm(IC(kk), N_);
        SCHED();
        if constexpr (n < f1 - f0) Sf[n] = LD(u32x4, xst[(f0 + n) & 3] + ((f0 + n) >> 2) * 1024);
        if constexpr (H) {
          if constexpr (hs < 15) {
            rd(IC(hs + 1), N_);
            if constexpr (n == 7) rd(IC(hs + 1), IC(8));
          }
        } else {
          if constexpr (hs < 15) {
            if constexpr (n == 0) rd(IC(hs + 1), IC(0));
            rd(IC(hs + 1), IC(n + 1));
          }
          constexpr int B = (hs + 1) / 2;
          if constexpr (hs == 0) {
            if constexpr (n < 6) stream(IC(0), N_);
            if constexpr (n >= 4) stream(IC(0), IC(n + 2));
          } else if constexpr (hs == 15) {
            stream(IC(B), N_);
          } else {
            if constexpr (n < 6 && !(n & 1)) stream(IC(B), IC((1 - kk) * 3 + n / 2));
            if constexpr (kk == 1 && (n == 1 || n == 5)) stream(IC(B), IC(6 + n / 4));
            if constexpr (kk == 0 && n == 1) stream(IC(B), IC(8));
          }
        }
        if constexpr (hs == 13 && n == 3) xres = LD(u32x4, xra);
        if constexpr (hs == 13 && n == 2) {
          float d0 = sA.x, d1 = sA.y, d2 = sA.z;
          PINV(d0); PINV(d1); PINV(d2);
          snm = mul1(sA.w, -1.0f / D);
          srv = mul1(pp == 0 ? d0 : pp == 1 ? d1 : d2, 1.0f / D);
        }
        if constexpr (hs == 13 && n == 4) srv = fmaxf(srv - snm * snm, 0.0f) + EPS;
        if constexpr (hs == 13 && n == 5) srv = __builtin_amdgcn_rsqf(srv);
        if constexpr (hs == 14 && n == 0) {
          if (q == qp && pp != 3) { LD(float, fin) = snm; LD(float, fin + 384) = srv; }
        }
        if constexpr (hs == 15) {
          if constexpr (n == 0) {
#pragma unroll
            for (int a = 0; a < 2; ++a) {
              fq[a][0] = LD(float, mrL + 384 + (a + 1) * 64);
              rk[a][0] = LD(float, rk0a + a * 64);
              rk[a][1] = LD(float, rk1a + a * 64);
            }
          }
          if constexpr (n == 1) {
#pragma unroll
            for (int a = 0; a < 3; ++a) { mL[a] = LD(float, mrL + a * 64); mR[a] = LD(f32x4, mrR + a * 64); }
          }
          if constexpr (n == 4) {
#pragma unroll
            for (int a = 0; a < 2; ++a) {
              const float cq = mul1(fq[a][0], 0.25f * 1.44269504088896f);
              fq[a][0] = mul1(rk[a][0], cq);
              fq[a][1] = mul1(rk[a][1], cq);
            }
          }
        }
        SCHED();
      });
      sfor<f1 - f0>([&](auto I_) {
        sA = mfma32(Sf[I_.value], Sf[I_.value], sA);
        SCHED();
      });
      if constexpr (kk == 0) {
        constexpr int B = s + 1;
        if constexpr (B <= 7) TD(2 * B - 1);
        if constexpr (H) {
          if constexpr (B == 1) wait_vm<0>();
        } else {
          if constexpr (B >= 2 && B < 8) wait_vm<(B == 2 ? 8 : B == 3 ? 9 : B == 4 ? 8 : 6)>();
        }
        barrier_lgkm<(hs == 14 ? 0 : H ? 2 : 1)>();
        if constexpr (B <= 7) TD(2 * B);
        SCHED();
      }
    });
    if constexpr (H) {
      LD(f32x4, exa) = aK[2];
      LD(f32x4, exa + 1024) = aV[2];
      if (r16 == 0) LD(f32x4, csa + 64) = aK[0];
      if (q == 0) LD(float, csv) = aV[0].x;
    } else {
      if (r16 == 0) LD(f32x4, csa) = aC;
    }
    BARRIER();
    if constexpr (!H) {
      aK[0] = LD(f32x4, exa);
      aV[0] = LD(f32x4, exa + 1024);
    }
  };
  if (heavy) p1_loop(std::true_type{}); else p1_loop(std::false_type{});
  TS(2);
  constexpr int NREG = NPRE2;
  u32x4 B1[8][4], B2[7][4], Bo[4][4];
  u32x4 hreg;
  auto bo_read = [&](int k) {
#pragma unroll
    for (int n = 0; n < 4; ++n) Bo[k][n] = trfrag(trO[n] + k * 4096);
  };
  float bias;
  u32x4 bb;
  {
    const f32x4 csQ = LD(f32x4, csa), csK = LD(f32x4, csa + 64);
    const float csV = LD(float, csv);
    bias = blo((u32)LD(u16, L_B2 + s_ * 2));
    bb = LD(u32x4, L_B1 + (w & 1) * 64 + q * 16);
    w1_piece(IC(0), IC(0), IC(SA));
    u32x2 qf[2], kf[3], vf[3];
#pragma unroll
    for (int a = 0; a < 2; ++a) {
      const f32x4 v = aQ[a] + mL[a + 1] * csQ;
      qf[a] = u32x2{pk(v[0], v[1]), pk(v[2], v[3])};
    }
    w1_piece(IC(0), IC(1), IC(SA));
#pragma unroll
    for (int a = 0; a < 3; ++a) {
      const f32x4 v = aK[a] + mL[a] * csK;
      kf[a] = u32x2{pk(v[0], v[1]), pk(v[2], v[3])};
    }
    w1_piece(IC(0), IC(2), IC(SA));
#pragma unroll
    for (int a = 0; a < 3; ++a) {
      const f32x4 v = aV[a] + mR[a] * csV;
      vf[a] = u32x2{pk(v[0], v[1]), pk(v[2], v[3])};
    }
    SCHED();
    f32x4 sp[2], so[2];
#pragma unroll
    for (int a = 0; a < 2; ++a) {
      sp[a] = mfma16(kf[a], qf[a], z4);
      so[a] = mfma16(kf[a + 1], qf[a], z4);
    }
    w1_piece(IC(0), IC(3), IC(SA));
    wait_vm<4>();
    BARRIER();
    bo_read(0);
    SCHED();
    float lg[2][2], mx[2];
#pragma unroll
    for (int a = 0; a < 2; ++a) {
      const float ev0 = own0 ? so[a].x : sp[a].x, od0 = own0 ? so[a].y : sp[a].y;
      const float ev1 = own1 ? so[a].z : sp[a].z, od1 = own1 ? so[a].w : sp[a].w;
      lg[a][0] = (odd ? od0 : ev0) * fq[a][0];
      lg[a][1] = (odd ? od1 : ev1) * fq[a][1];
      if (a == 0) {
        const bool first = (t == 0) && heavy;
        lg[a][0] = (first && !own0) ? -1.44269504088896e9f : lg[a][0];
        lg[a][1] = (first && !own1) ? -1.44269504088896e9f : lg[a][1];
      }
      mx[a] = vmax(lg[a][0], lg[a][1]);
    }
    w1_piece(IC(1), IC(0), IC(SB));
    bo_read(1);
    SCHED();
    allred2<true>(mx[0], mx[1]);
    float ee[2][2], sm[2];
#pragma unroll
    for (int a = 0; a < 2; ++a) {
      ee[a][0] = __builtin_amdgcn_exp2f(lg[a][0] - mx[a]);
      ee[a][1] = __builtin_amdgcn_exp2f(lg[a][1] - mx[a]);
      sm[a] = ee[a][0] + ee[a][1];
    }
    w1_piece(IC(1), IC(1), IC(SB));
    bo_read(2);
    SCHED();
    allred2<false>(sm[0], sm[1]);
    u32x4 pf[2];
#pragma unroll
    for (int a = 0; a < 2; ++a) {
      const float inv = __builtin_amdgcn_rcpf(sm[a]);
      const u32 pw = pk(ee[a][0] * rk[a][0] * inv, ee[a][1] * rk[a][1] * inv);
      const u32 p0 = __builtin_amdgcn_perm(0u, pw, sel0), p1 = __builtin_amdgcn_perm(0u, pw, sel1);
      pf[a] = u32x4{own0 ? 0u : p0, own1 ? 0u : p1, own0 ? p0 : 0u, own1 ? p1 : 0u};
      if (a == 0) w1_piece(IC(1), IC(2), IC(SB));
    }
    f32x4 o[2];
#pragma unroll
    for (int a = 0; a < 2; ++a) o[a] = mfma32(u32x4{vf[a].x, vf[a].y, vf[a + 1].x, vf[a + 1].y}, pf[a], z4);
    w1_piece(IC(1), IC(3), IC(SB));
    auto s0 = __builtin_amdgcn_permlane16_swap(pk(o[0][0], o[0][1]), pk(o[1][0], o[1][1]), false, false);
    auto s1 = __builtin_amdgcn_permlane16_swap(pk(o[0][2], o[0][3]), pk(o[1][2], o[1][3]), false, false);
    const u32x4 ov = {s0[0], s1[0], s0[1], s1[1]};
    st_sc1(ov, rs_ws, ooff, WS_O + (t * 8 + c1) * 8192 + ta * 4096 + hh * 512);
  }
  TS(3);
  bo_read(3);
  w1_issue(IC(2), IC(SC));
  u32 trS[4], lane16 = lane * 16;
#pragma unroll
  for (int n = 0; n < 4; ++n) { trS[n] = (trN ^ (n << 5)) + stg + SA; PINV(trS[n]); }
  auto rd4 = [&](u32x4(&B)[4], u32 slot) {
#pragma unroll
    for (int n = 0; n < 4; ++n) B[n] = trfrag(trS[n] + (slot - SA));
  };
  auto lgkm0 = [&]() { SCHED(); WAIT_LGKM0(); SCHED(); };
  u32 w2E = (lane >> 3) * (S * 2) + swzE, w2O = (lane >> 3) * (S * 2) + (swzE ^ 64) + 8 * (S * 2) - 1024;
  const u32 w2s = w * 224 * (S * 2) + t * 128;
  auto w2_issue = [&](auto K_, auto LC_) { dma16x4<8 * S * 2, LC_.value>(stg, w2E, w2O, rs_w2, w2s + K_.value * 32 * (S * 2)); };
  u32 redw = q * 256 + ((r16 ^ ((q >> 1) << 1)) << 4);
  u32 redr = (((gq >> 1) * 4 + (s_ >> 4)) * 1024) + ((gq & 1) * 32 + ((s_ & 15) ^ (gq << 1))) * 16;
  PINV(redw); PINV(redr); PINV(lane16);
  wait_vm<4>();
  BARRIER();
  if (w == 0 && lane < 8) put8(ws + (WS_F1 + t * 1024 + c1 * 8), lane * 128, tag);
  TS(4);
  {
    rd4(B1[0], SA); rd4(B1[1], SB);
    lgkm0();
    w1_issue(IC(3), IC(SA));
    poll_tag<2>(ws + (WS_F1 + g * 128 + hh * 16), lane, tag);
    TS(5);
    u32x4 ofr[8];
    {
      const u32 os0 = WS_O + (t * 8 + 2 * hh) * 8192 + th * 4096, os1 = os0 + 8192;
      asm volatile(
          "buffer_load_dwordx4 %0, %8, %9, %10 offen sc1\n\t"
          "buffer_load_dwordx4 %1, %8, %9, %10 offen offset:1024 sc1\n\t"
          "buffer_load_dwordx4 %2, %8, %9, %11 offen sc1\n\t"
          "buffer_load_dwordx4 %3, %8, %9, %11 offen offset:1024 sc1\n\t"
          "buffer_load_dwordx4 %4, %8, %9, %10 offen offset:2048 sc1\n\t"
          "buffer_load_dwordx4 %5, %8, %9, %10 offen offset:3072 sc1\n\t"
          "buffer_load_dwordx4 %6, %8, %9, %11 offen offset:2048 sc1\n\t"
          "buffer_load_dwordx4 %7, %8, %9, %11 offen offset:3072 sc1"
          : "=&v"(ofr[0]), "=&v"(ofr[1]), "=&v"(ofr[2]), "=&v"(ofr[3]), "=&v"(ofr[4]), "=&v"(ofr[5]), "=&v"(ofr[6]), "=&v"(ofr[7])
          : "v"(lane16), "s"(rs_ws), "s"(os0), "s"(os1)
          : "memory");
    }
    rd4(B1[2], SC); rd4(B1[3], SA);
    SCHED();
    sfor<2>([&](auto TT_) {
      constexpr int tt = TT_.value;
      f32x4 acc[4];
#pragma unroll
      for (int n = 0; n < 4; ++n) acc[n] = z4;
      sfor<4>([&](auto K_) {
        constexpr int k = K_.value, i = tt * 4 + k;
        wait_tie<7 - i + i / 2>(ofr[i]);
#pragma unroll
        for (int n = 0; n < 4; ++n) acc[n] = mfma32(Bo[k][n], ofr[i], acc[n]);
        if constexpr (i & 1) w1_piece(IC(4), IC(i / 2), IC(SB));
        SCHED();
      });
#pragma unroll
      for (int n = 0; n < 4; ++n) LD(f32x4, L_RING + hh * 16384 + th * 2048 + ((redw ^ (n << 6)) + (n * 4 + tt) * 1024)) = acc[n];
    });
    TS(19);
    BARRIER();
    TS(6);
    f32x4 a0 = LD(f32x4, L_RING + redr), a1 = LD(f32x4, L_RING + redr + 256);
#pragma unroll
    for (int k = 1; k < 4; ++k) {
      a0 += LD(f32x4, L_RING + k * 16384 + redr);
      a1 += LD(f32x4, L_RING + k * 16384 + redr + 256);
    }
    hreg = u32x4{pk(blo(xres.x) + a0[0], bhi(xres.x) + a0[1]), pk(blo(xres.y) + a0[2], bhi(xres.y) + a0[3]),
                 pk(blo(xres.z) + a1[0], bhi(xres.z) + a1[1]), pk(blo(xres.w) + a1[2], bhi(xres.w) + a1[3])};
    const f32x4 h0 = {blo(hreg.x), bhi(hreg.x), blo(hreg.y), bhi(hreg.y)}, h1 = {blo(hreg.z), bhi(hreg.z), blo(hreg.w), bhi(hreg.w)};
    const f32x4 hs = h0 + h1, hq = h0 * h0 + h1 * h1;
    const float ps = sum8((hs.x + hs.y) + (hs.z + hs.w)), pq = sum8((hq.x + hq.y) + (hq.z + hq.w));
    if (gq == 0) st8_sc1(u32x2{min(f2u(ps), 0xfffffffeu), min(f2u(pq), 0xfffffffeu)}, rs_ws, s_ * 8, WS_ST + g * 512);
    TS(7);
    w1_issue(IC(5), IC(SC));
    if constexpr (S3N == 2) w1_issue(IC(6), IC(SA));
    if constexpr (SDELAY > 0) __builtin_amdgcn_s_sleep(SDELAY);
    u32x2 sv;
    for (int sp = 0; sp < SPIN_CAP; ++sp) {
      FENCE();
      sv = __builtin_amdgcn_raw_buffer_load_b64(rs_ws, gq * 512 + s_ * 8, WS_ST + t * 4096, 16);
      if (!__any(max(sv.x, sv.y) == 0xffffffffu)) break;
    }
    TS(8);
    rd4(B1[5], SC);
    {
      const float m = sum8(u2f(sv.x)) * (1.0f / D);
      const float v = fmaxf(sum8(u2f(sv.y)) * (1.0f / D) - m * m, 0.0f);
      const float r1 = __builtin_amdgcn_rsqf(v + EPS);
      const float r2 = __builtin_amdgcn_rsqf(v * r1 * r1 + EPS);
      const float r = r1 * r2;
      const f32x4 n0 = (h0 - m) * r, n1 = (h1 - m) * r;
      const u32x4 hn = {pk(n0[0], n0[1]), pk(n0[2], n0[3]), pk(n1[0], n1[1]), pk(n1[2], n1[3])};
      const u32 tb = SC + w * 4096;
      LD(u32x4, tb + (lane16 ^ ((q & 1) << 6))) = hn;
      FENCE();
      const u32x4 hf = trfrag(tb + qp * 128 + ((q * 32) ^ ((qp >> 1) << 6)) + pp * 8);
      st_sc1(hf, rs_ws, q * 1024 + r16 * 16, WS_HN + c * 262144 + (2 * t + th) * 4096 + hh * 256);
    }
    SCHED();
    w1_issue(IC(7), IC(SC));
    rd4(B1[4], SB);
    if constexpr (S3N == 2) rd4(B1[6], SA);
    wait_vm<4>();
    BARRIER();
    if (w == 0 && lane < 32) put8(ws + (WS_F2 + c * 256 + t * 8), lane * 2048, tag);
    TS(9);
  }

  if constexpr (S3N == 1) w1_issue(IC(6), IC(32768));
  w2_issue(IC(NREG), IC(SB));
  if constexpr (NREG >= 1) w2_issue(IC(0), IC(SA));
  if constexpr (NREG >= 2) w2_issue(IC(1), IC(0));
  if (p3) {
    const u32 mte = w >> 1, kp = w & 1;
    poll_tag<4>(ws + (WS_F2 + g * 256 + w * 32), lane, tag);
    TS(10);
    constexpr int RING = RING3;
    u32x4 A[RING];
    const u32 as = WS_HN + c * 262144 + w * 32768;
#pragma unroll
    for (int i = 0; i < RING; ++i) A[i] = ld_sc1(rs_ws, lane16 + (i >> 3) * 1024, as + (i & 7) * 4096);
    SCHED();
    rd4(B1[7], SC);
    if constexpr (S3N == 1) rd4(B1[6], 32768);
    if constexpr (NREG >= 1) rd4(B2[0], SA);
    if constexpr (NREG >= 2) rd4(B2[1], 0);
    BARRIER();
    SCHED();
#pragma unroll
    for (int mt = 0; mt < 4; ++mt) {
      if (mt == 0) __builtin_amdgcn_s_setprio(3);
      if (mt == 1) __builtin_amdgcn_s_setprio(2);
      if (mt == 2) __builtin_amdgcn_s_setprio(1);
      if (mt == 3) __builtin_amdgcn_s_setprio(0);
      f32x4 acc[4];
#pragma unroll
      for (int n = 0; n < 4; ++n) acc[n] = z4;
#pragma unroll
      for (int k = 0; k < 8; ++k) {
        const int i = mt * 8 + k;
        const u32x4 cur = A[i % RING];
#pragma unroll
        for (int n = 0; n < 4; ++n) acc[n] = mfma32(B1[k][n], cur, acc[n]);
        if (i + RING < 32) A[i % RING] = ld_sc1(rs_ws, lane16 + ((i + RING) >> 3) * 1024, as + ((i + RING) & 7) * 4096);
        if (i == 27) rd4(B2[NREG], SB);
        SCHED();
      }
      if (mt == 3) {
        WAIT_LGKM0();
        w2_issue(IC(NREG + 1), IC(SB));
      }
#pragma unroll
      for (int n = 0; n < 4; ++n) LD(f32x4, w * 16384 + lane16 + (mt * 4 + n) * 1024) = acc[n];
    }
    TS(16);
    BARRIER();
    TS(11);
    if (j == 0 && c == 0 && tid == 0) *(GAS u64*)(ws + WS_EPOCH) = epoch + 1;
    {
      f32x4 s0 = {blo(bb.x), bhi(bb.x), blo(bb.y), bhi(bb.y)}, s1 = {blo(bb.z), bhi(bb.z), blo(bb.w), bhi(bb.w)};
      const u32 ra = (mte * 4 + 2 * kp + (q >> 1)) * 1024 + ((q & 1) * 32 + r16) * 16;
#pragma unroll
      for (int ww = 0; ww < 8; ++ww) {
        s0 += LD(f32x4, ww * 16384 + ra);
        s1 += LD(f32x4, ww * 16384 + ra + 256);
      }
      float v[8] = {s0[0], s0[1], s0[2], s0[3], s1[0], s1[1], s1[2], s1[3]};
#pragma unroll
      for (int e = 0; e < 8; ++e) {
        const float z = v[e] * (v[e] * v[e] * (-0.044715f * 2.302208198f) - 2.302208198f);
        v[e] = v[e] * __builtin_amdgcn_rcpf(1.0f + __builtin_amdgcn_exp2f(z));
      }
      const u32x4 uf = {pk(v[0], v[1]), pk(v[2], v[3]), pk(v[4], v[5]), pk(v[6], v[7])};
      st_sc1(uf, rs_ws, lane16, WS_U + c * (56 * 4096) + (2 * j + kp) * 4096 + mte * 1024);
    }
    BARRIER();
    w2_issue(IC(NREG + 2), IC(SA));
    wait_vm<4>();
    BARRIER();
    if (w == 0 && lane < 32) put8(ws + (WS_F3 + c * 256 + j * 8), lane * 2048, tag);
  } else {
    wait_vm<0>();
    if constexpr (NREG >= 1) rd4(B2[0], SA);
    if constexpr (NREG >= 2) rd4(B2[1], 0);
    rd4(B2[NREG], SB);
    lgkm0();
    w2_issue(IC(NREG + 1), IC(SB));
    w2_issue(IC(NREG + 2), IC(SA));
    sfor<4 - NREG>([&](auto i) { w2_issue(IC(NREG + 3 + i.value), IC(i.value * 32768)); });
  }
  TS(12);

  {
    constexpr int NR = 4 - NREG;
    if (p3) sfor<NR>([&](auto i) { w2_issue(IC(NREG + 3 + i.value), IC(i.value * 32768)); });
    TS(13);
    poll_tag<4>(ws + (WS_F3 + g * 256 + ((7 * w) >> 1) * 8), lane, tag);
    TS(14);
    constexpr int RING = RING4;
    u32x4 A[RING];
    const u32 as = WS_U + c * (56 * 4096) + w * (7 * 4096);
#pragma unroll
    for (int i = 0; i < RING; ++i) A[i] = ld_sc1(rs_ws, lane16 + (i / 7) * 1024, as + (i % 7) * 4096);
    SCHED();
    rd4(B2[NREG + 1], SB);
    rd4(B2[NREG + 2], SA);
    sfor<NR>([&](auto i) { rd4(B2[NREG + 3 + i.value], i.value * 32768); });
    BARRIER();
    SCHED();
#pragma unroll
    for (int mt = 0; mt < 4; ++mt) {
      if (mt == 0) __builtin_amdgcn_s_setprio(3);
      if (mt == 1) __builtin_amdgcn_s_setprio(2);
      if (mt == 2) __builtin_amdgcn_s_setprio(1);
      if (mt == 3) __builtin_amdgcn_s_setprio(0);
      f32x4 acc[4];
#pragma unroll
      for (int n = 0; n < 4; ++n) acc[n] = z4;
#pragma unroll
      for (int k = 0; k < 7; ++k) {
        const int i = mt * 7 + k;
        const u32x4 cur = A[i % RING];
#pragma unroll
        for (int n = 0; n < 4; ++n) acc[n] = mfma32(cur, B2[k][n], acc[n]);
        if (i + RING < 28) A[i % RING] = ld_sc1(rs_ws, lane16 + ((i + RING) / 7) * 1024, as + ((i + RING) % 7) * 4096);
        SCHED();
      }
#pragma unroll
      for (int n = 0; n < 4; ++n) LD(f32x4, w * 16384 + (redw ^ (mt << 6)) + (mt * 4 + n) * 1024) = acc[n];
    }
    f32x4 s0 = {blo(hreg.x) + bias, bhi(hreg.x) + bias, blo(hreg.y) + bias, bhi(hreg.y) + bias};
    f32x4 s1 = {blo(hreg.z) + bias, bhi(hreg.z) + bias, blo(hreg.w) + bias, bhi(hreg.w) + bias};
    TS(17);
    BARRIER();
    TS(18);
#pragma unroll
    for (int ww = 0; ww < 8; ++ww) {
      s0 += LD(f32x4, ww * 16384 + redr);
      s1 += LD(f32x4, ww * 16384 + redr + 256);
    }
    const u32x4 ov = {pk(s0[0], s0[1]), pk(s0[2], s0[3]), pk(s1[0], s1[1]), pk(s1[2], s1[3])};
    st_sc1(ov, rs_out, s_ * 1024 + gq * 16, t * 65536 + c * 128);
  }
  asm volatile(".Lfb_end_mark:" ::: "memory");
}

extern "C" void kernel_launch(void* const* d_in, const int* in_sizes, int n_in, void* d_out, int out_size, void* d_ws, size_t ws_size,
                              hipStream_t stream) {
  (void)in_sizes; (void)n_in; (void)out_size; (void)ws_size;
  (void)hipFuncSetAttribute((const void*)fused_block, hipFuncAttributeMaxDynamicSharedMemorySize, L_TOTAL);
  fused_block<<<256, 512, L_TOTAL, stream>>>((const u16*)d_in[0], (const u16*)d_in[1], (char*)d_ws, (const u16*)d_in[2], (const u16*)d_in[4],
                                             (const u16*)d_in[6], (const u16*)d_in[5], (const u16*)d_in[7], (u16*)d_out);
}
```

```cpp
#define PROBE 1
#include <hip/hip_runtime.h>
#include <utility>
#pragma clang diagnostic ignored "-Wint-to-pointer-cast"

typedef unsigned short u16;
typedef unsigned u32;
typedef unsigned long long u64;
typedef __bf16 bf16x8 __attribute__((ext_vector_type(8)));
typedef __bf16 bf16x2 __attribute__((ext_vector_type(2)));
typedef short s16x4 __attribute__((ext_vector_type(4)));
typedef float f32x4 __attribute__((ext_vector_type(4)));
typedef float f32x2 __attribute__((ext_vector_type(2)));
typedef u32 u32x4 __attribute__((ext_vector_type(4)));
typedef u32 u32x2 __attribute__((ext_vector_type(2)));
#define LAS __attribute__((address_space(3)))
#define GAS __attribute__((address_space(1)))
#define DI __device__ __forceinline__
typedef __amdgpu_buffer_rsrc_t rsrc_t;

#define STAMP_TID 0
#define SDELAY 0
#define S3N 2
#define RING3 7
#define RING4 8
#define NPRE2 2

constexpr int S = 2048, D = 512, F = 1792;
constexpr float EPS = 1e-5f;
constexpr u32 WS_EPOCH = 0;
constexpr u32 WS_F1 = 4096;
constexpr u32 WS_F2 = WS_F1 + 256 * 128;
constexpr u32 WS_F3 = WS_F2 + 256 * 256;
constexpr u32 WS_F4 = WS_F3 + 256 * 256;
constexpr u32 WS_ST = WS_F4 + 256 * 256;
constexpr u32 WS_O = 2u << 20;
constexpr u32 WS_HN = WS_O + (2u << 20);
constexpr u32 WS_U = WS_HN + (2u << 20);
constexpr u32 WS_END = WS_U + (2u << 20);
constexpr u32 WS_STAMP = WS_END;
constexpr u32 L_RING = 0;
constexpr u32 L_X = 81920;
constexpr u32 L_MISC = 73728;
constexpr u32 L_PART = L_MISC;
constexpr u32 L_CS = L_MISC + 4096;
constexpr u32 L_MR = L_MISC + 5120;
constexpr u32 L_SCR = L_MISC + 6144;
constexpr u32 L_B1 = L_MISC + 7680;
constexpr u32 L_B2 = L_MISC + 7936;
constexpr u32 L_TOTAL = 163840;
constexpr int SPIN_CAP = 1 << 18;

DI void dma4(u32 lds, u32 voff, rsrc_t rs, u32 soff) {
  asm volatile("s_mov_b32 m0, %0\n\ts_nop 0\n\tbuffer_load_dword %1, %2, %3 offen lds" ::"s"(lds), "v"(voff), "s"(rs), "s"(soff) : "memory");
}
template <int LC, int SC>
DI void dma16c(u32 lds, u32 voff, rsrc_t rs, u32 soff) {
  u32 tmp;
  asm volatile("s_add_u32 m0, %1, %5\n\ts_add_u32 %0, %4, %6\n\tbuffer_load_dwordx4 %2, %3, %0 offen lds"
               : "=&s"(tmp) : "s"(lds), "v"(voff), "s"(rs), "s"(soff), "n"(LC), "n"(SC) : "memory", "scc");
}
template <int LC, int SC>
DI void dma16m(u32 lds, u32 voff, rsrc_t rs, u32 soff, u64 mask) {
  u32 tmp;
  asm volatile("s_add_u32 m0, %1, %5\n\ts_add_u32 %0, %4, %6\n\ts_mov_b64 exec, %7\n\tbuffer_load_dwordx4 %2, %3, %0 offen lds\n\ts_mov_b64 exec, -1"
               : "=&s"(tmp) : "s"(lds), "v"(voff), "s"(rs), "s"(soff), "n"(LC), "n"(SC), "s"(mask) : "memory", "scc");
}
template <int STEP, int LC>
DI void dma16x4(u32 lds, u32 voffE, u32 voffO, rsrc_t rs, u32 soff) {
  u32 tmp;
  asm volatile(
      "s_add_u32 m0, %1, %7\n\t"
      "s_add_u32 %0, %5, %6\n\t"
      "buffer_load_dwordx4 %2, %4, %5 offen lds\n\t"
      "buffer_load_dwordx4 %3, %4, %5 offen offset:1024 lds\n\t"
      "buffer_load_dwordx4 %2, %4, %0 offen offset:2048 lds\n\t"
      "buffer_load_dwordx4 %3, %4, %0 offen offset:3072 lds"
      : "=&s"(tmp)
      : "s"(lds), "v"(voffE), "v"(voffO), "s"(rs), "s"(soff), "n"(2 * STEP - 2048), "n"(LC)
      : "memory", "scc");
}
template <int N> DI void wait_vm() { asm volatile("s_waitcnt vmcnt(%0)" ::"n"(N) : "memory"); }
template <int N> DI void wait_tie(u32x4& v) { asm volatile("s_waitcnt vmcnt(%1)" : "+v"(v) : "n"(N) : "memory"); }
template <int N> DI void barrier_lgkm() { asm volatile("s_waitcnt lgkmcnt(%0)\n\ts_barrier" ::"n"(N) : "memory"); }
#define WAIT_LGKM0() asm volatile("s_waitcnt lgkmcnt(0)" ::: "memory")
#define BARRIER() barrier_lgkm<0>()
#define SCHED() __builtin_amdgcn_sched_barrier(0)
#define FENCE() asm volatile("" ::: "memory")
#define PINV(x) asm volatile("" : "+v"(x))
#define PINS(x) asm volatile("" : "+s"(x))
#define IC(n) std::integral_constant<int, (n)>{}
#define LD(T, a) (*(LAS T*)(a))

DI f32x4 mfma32(u32x4 a, u32x4 b, f32x4 c) {
  return __builtin_amdgcn_mfma_f32_16x16x32_bf16(__builtin_bit_cast(bf16x8, a), __builtin_bit_cast(bf16x8, b), c, 0, 0, 0);
}
DI f32x4 mfma16(u32x2 a, u32x2 b, f32x4 c) {
  return __builtin_amdgcn_mfma_f32_16x16x16bf16_1k(__builtin_bit_cast(s16x4, a), __builtin_bit_cast(s16x4, b), c, 0, 0, 0);
}
DI u32x2 trd(u32 addr) { return __builtin_bit_cast(u32x2, __builtin_amdgcn_ds_read_tr16_b64_v4i16((LAS s16x4*)(addr))); }
DI u32x4 trfrag(u32 a) {
  u32x2 lo = trd(a), hi = trd(a + 512);
  return u32x4{lo.x, lo.y, hi.x, hi.y};
}
DI u32 pk(float a, float b) {
  f32x2 v = {a, b};
  return __builtin_bit_cast(u32, __builtin_convertvector(v, bf16x2));
}
DI float blo(u32 v) { return __builtin_bit_cast(float, v << 16); }
DI float bhi(u32 v) { return __builtin_bit_cast(float, v & 0xffff0000u); }
DI u32 f2u(float f) { return __builtin_bit_cast(u32, f); }
DI float u2f(u32 u) { return __builtin_bit_cast(float, u); }
DI float vmax(float a, float b) { return __builtin_elementwise_maximum(a, b); }
DI float mul1(float a, float b) { float r = a * b; PINV(r); return r; }
template <bool MAX>
DI void allred2(float& a, float& b) {
  auto r1 = __builtin_amdgcn_permlane16_swap(f2u(a), f2u(b), false, false);
  float t = MAX ? vmax(u2f(r1[0]), u2f(r1[1])) : u2f(r1[0]) + u2f(r1[1]);
  auto r2 = __builtin_amdgcn_permlane32_swap(f2u(t), f2u(t), false, false);
  float u = MAX ? vmax(u2f(r2[0]), u2f(r2[1])) : u2f(r2[0]) + u2f(r2[1]);
  auto r3 = __builtin_amdgcn_permlane16_swap(f2u(u), f2u(u), false, false);
  a = u2f(r3[0]);
  b = u2f(r3[1]);
}
template <int CTRL> DI float dppadd(float v) { return v + __builtin_amdgcn_update_dpp(0.0f, v, CTRL, 0xf, 0xf, true); }
DI float sum8(float v) {
  v = dppadd<0xB1>(v);
  v = dppadd<0x4E>(v);
  v = dppadd<0x141>(v);
  return v;
}
DI u32x4 ld_sc1(rsrc_t rs, u32 voff, u32 soff) { return __builtin_amdgcn_raw_buffer_load_b128(rs, voff, soff, 16); }
DI void st_sc1(u32x4 v, rsrc_t rs, u32 voff, u32 soff) { __builtin_amdgcn_raw_buffer_store_b128(v, rs, voff, soff, 16); }
DI void st8_sc1(u32x2 v, rsrc_t rs, u32 voff, u32 soff) { __builtin_amdgcn_raw_buffer_store_b64(v, rs, voff, soff, 16); }

DI void put8(char* base, u32 off, u64 v) { (void)__hip_atomic_exchange((GAS u64*)(base + off), v, __ATOMIC_RELAXED, __HIP_MEMORY_SCOPE_AGENT); }
DI u64 get8(char* base, u32 off) {
  u64 zero = 0;
  PINV(zero);
  return __hip_atomic_fetch_add((GAS u64*)(base + off), zero, __ATOMIC_RELAXED, __HIP_MEMORY_SCOPE_AGENT);
}
template <int N>
DI void poll_tag(char* base, u32 lane, u64 tag) {
  if (lane < N) {
    for (int sp = 0; sp < SPIN_CAP; ++sp)
      if (__all(get8(base, lane * 8) == tag)) break;
  }
  FENCE();
}
template <typename Fn, int... I> DI void sfor_impl(Fn&& f, std::integer_sequence<int, I...>) { (f(std::integral_constant<int, I>{}), ...); }
template <int N, typename Fn> DI void sfor(Fn&& f) { sfor_impl(f, std::make_integer_sequence<int, N>{}); }

#define TS(i) do { if ((i) == PROBE) { SCHED(); probe_t = __builtin_amdgcn_s_memrealtime(); SCHED(); } } while (0)
#define TD(i)

constexpr int stat_f0(int hs) {
  constexpr int v[14] = {0, 1, 2, 4, 5, 6, 8, 9, 10, 12, 13, 14, 15, 16};
  return v[hs < 13 ? hs : 13];
}

__global__ __launch_bounds__(512, 2) void fused_block(const u16* __restrict__ x, const u16* __restrict__ wqkv, char* __restrict__ ws,
                                                      const u16* __restrict__ wo, const u16* __restrict__ w1, const u16* __restrict__ w2,
                                                      const u16* __restrict__ b1, const u16* __restrict__ b2, u16* __restrict__ out) {
  u64 probe_t = 0;
  TS(0);
  const u32 tid = threadIdx.x, lane = tid & 63;
  const u32 w = __builtin_amdgcn_readfirstlane(tid >> 6);
  const u32 b = blockIdx.x, xcd = b & 7, idx = b >> 3;
  const u32 c = (xcd & 1) * 4 + (idx & 3), tl = idx >> 2, sq = xcd >> 1, t = sq * 8 + tl;
  const u32 c1 = c ^ 1;
  const u32 hh = w & 3, th = w >> 2;
  const bool heavy = w >= 4;
  const u32 r16 = lane & 15, q = lane >> 4;

  const rsrc_t rs_x = __builtin_amdgcn_make_buffer_rsrc((void*)x, 0, S * D * 2, 0x00020000);
  const rsrc_t rs_wqkv = __builtin_amdgcn_make_buffer_rsrc((void*)wqkv, 0, D * 3 * D * 2, 0x00020000);

  const u32 xrw = 4 * hh + q;
  const u32 xchunk = (r16 ^ xrw) << 4;
  const u32 xv = xrw * 1024 + xchunk;
  const u32 xv0 = t == 0 ? xchunk : xv;
  const u32 xs0 = t == 0 ? 0 : t * 65536 - 16384;
  const u64 xmask = hh == 0 ? ~0xffffull : ~0ull;
  const u32 swzE = ((lane & 7) ^ ((q & 1) << 1)) << 4;
  const u32 swzW = swzE ^ ((hh & 1) << 6);
  const u32 wv = (hh * 8 + (lane >> 3)) * 3072 + swzW;
  auto dma_x = [&](auto A_, auto Q_) {
    constexpr int a = A_.value, qt = Q_.value;
    if constexpr (a == 0) dma16m<L_X + qt * 1024, qt * 256>(hh * 4096, xv0, rs_x, xs0, xmask);
    else dma16c<L_X + a * 16384 + qt * 1024, (a - 1) * 16384 + qt * 256>(hh * 4096, xv, rs_x, t * 65536);
  };
  auto dma_w = [&](auto S_, auto I_) {
    constexpr int s = S_.value, seg = I_.value % 3, hi = I_.value / 3;
    dma16c<L_RING + ((s + 1) % 3) * 24576 + hi * 12288 + seg * 4096, (s * 64 + hi * 32) * 3072 + seg * 1024>(hh * 1024, wv, rs_wqkv, c1 * 128);
  };
  asm volatile("s_nop 4" ::: "memory");
  if (heavy) {
    sfor<6>([&](auto i) { dma_w(IC(0), i); });
    sfor<6>([&](auto i) { dma_w(IC(1), i); });
  } else {
    sfor<5>([&](auto a) { dma_x(a, IC(0)); });
    sfor<5>([&](auto a) { dma_x(a, IC(1)); });
  }
  const u32 g = t * 8 + c;
  const u32 tidle = (3u - ((u32)(size_t)w2 >> 7)) & 7u;
  const bool p3 = tl != tidle;
  const u32 j = sq * 7 + (tl > tidle ? tl - 1 : p3 ? tl : 0);
  const rsrc_t rs_wo = __builtin_amdgcn_make_buffer_rsrc((void*)wo, 0, D * D * 2, 0x00020000);
  const rsrc_t rs_w1 = __builtin_amdgcn_make_buffer_rsrc((void*)w1, 0, p3 ? S * F * 2 : 0, 0x00020000);
  const rsrc_t rs_w2 = __builtin_amdgcn_make_buffer_rsrc((void*)w2, 0, F * S * 2, 0x00020000);
  const rsrc_t rs_ws = __builtin_amdgcn_make_buffer_rsrc((void*)ws, 0, WS_END, 0x00020000);
  const rsrc_t rs_out = __builtin_amdgcn_make_buffer_rsrc((void*)out, 0, S * D * 2, 0x00020000);
  if (w == 7) {
    u64 pc;
    u32 nbytes;
    asm volatile("s_getpc_b64 %0\n.Lfb_pc_mark:\n\ts_mov_b32 %1, .Lfb_end_mark-.Lfb_pc_mark" : "=s"(pc), "=s"(nbytes));
    const rsrc_t rs_pc = __builtin_amdgcn_make_buffer_rsrc((void*)(pc & ~(u64)15), 0, nbytes, 0x00020000);
    dma16c<L_SCR, 0>(0, lane * 16, rs_pc, idx * 1024);
  }
  if (w == 5) {
    const u32 cs = (3u - ((u32)(size_t)wqkv >> 7)) & 7u;
    if ((cs >> 2) == (c1 >> 2) && cs != c1) dma4(L_SCR, lane * 3072, rs_wqkv, tl * 64 * 3072 + ((c1 - cs - 1) & 3) * 1024 + cs * 128);
  }
  if (w == 6) {
    const rsrc_t rs_b1 = __builtin_amdgcn_make_buffer_rsrc((void*)b1, 0, F * 2, 0x00020000);
    const rsrc_t rs_b2 = __builtin_amdgcn_make_buffer_rsrc((void*)b2, 0, S * 2, 0x00020000);
    dma4(L_B1, lane * 4, rs_b1, j * 128);
    dma4(L_B2, lane * 4, rs_b2, t * 128);
    st8_sc1(u32x2{~0u, ~0u}, rs_ws, lane * 8, WS_ST + g * 512);
  }
  u32x4 ones = {0x3f803f80u, 0x3f803f80u, 0x3f803f80u, 0x3f803f80u};
  PINV(ones);
  if (w == 5) LD(u32x4, L_X + q * 1024 + r16 * 16) = ones;
  u64 epoch;
  asm volatile("s_load_dwordx2 %0, %1, 0x0\n\ts_waitcnt lgkmcnt(0)" : "=s"(epoch) : "s"(ws));
  u64 tag = (epoch + 1) * 0x9E3779B97F4A7C15ull + 0x632BE59BD9B4E019ull;
  PINS(tag);

  const u32 ta = heavy ? 0u : 1u;
  const u32 qp = r16 >> 2, pp = lane & 3;
  u32 trN = (8 * q + qp) * 128 + ((((qp >> 1) & 1) | ((q & 1) << 1)) << 5) + 8 * pp;
  u32 w1E = (lane >> 3) * (F * 2) + swzE, w1O = (lane >> 3) * (F * 2) + (swzE ^ 64) + 8 * (F * 2) - 1024;
  const u32 k0 = 4 * q + (r16 & 1);
  const bool own0 = k0 <= r16, own1 = k0 + 2 <= r16, odd = r16 & 1;
  u32 mrL = L_MR + ta * 128 + r16 * 4, mrR = L_MR + ta * 128 + q * 16;
  u32 rk0a = L_MR + 384 + ta * 128 + (k0 + (own0 ? 16 : 0)) * 4, rk1a = L_MR + 384 + ta * 128 + (k0 + 2 + (own1 ? 16 : 0)) * 4;
  u32 sel0 = odd ? 0x01000c0cu : 0x0c0c0100u, sel1 = odd ? 0x03020c0cu : 0x0c0c0302u;
  u32 csa = L_CS + hh * 192 + q * 16, csv = L_CS + hh * 192 + 128 + r16 * 4;
  u32 exa = L_X + hh * 2048 + lane * 16;
  u32 ooff = (q & 1) * 2048 + ((q >> 1) * 16 + r16) * 16;
  const u32 s_ = tid >> 3, gq = tid & 7;
  u32 xra;
  {
    const u32 row = 16 + s_, ch = c * 8 + gq;
    xra = L_X + (row >> 2) * 4096 + (ch >> 4) * 1024 + (row & 3) * 256 + (((ch & 15) ^ (row & 15)) << 4);
  }
  PINV(trN); PINV(w1E); PINV(w1O); PINV(mrL); PINV(mrR); PINV(rk0a); PINV(rk1a); PINV(sel0); PINV(sel1); PINV(csa); PINV(csv); PINV(exa);
  PINV(ooff); PINV(xra);
  u32 trO[4];
#pragma unroll
  for (int n = 0; n < 4; ++n) { trO[n] = (trN ^ (n << 5)) + hh * 16384; PINV(trO[n]); }
  constexpr u32 rot1 = 0;
  u32 w1s = w * 256 * (F * 2) + j * 128, stg = w * 4096;
  PINS(w1s); PINS(stg);
  auto w1_issue = [&](auto K_, auto LC_) {
    dma16x4<8 * F * 2, LC_.value>(stg, w1E, w1O, rs_w1, w1s + ((K_.value + rot1) & 7) * 32 * (F * 2));
  };
  auto w1_piece = [&](auto K_, auto P_, auto LC_) {
    constexpr int p = P_.value;
    SCHED();
    dma16c<LC_.value + p * 1024, (K_.value * 32 + (p >> 1) * 16) * (F * 2) + (p & 1) * 1024>(stg, (p & 1) ? w1O : w1E, rs_w1, w1s);
    SCHED();
  };
  constexpr u32 SC = 65536, SA = 98304, SB = 131072;

  f32x4 aQ[2], aK[3], aV[3];
  const f32x4 z4 = {0, 0, 0, 0};
  u32x4 xres;
  float mL[3], fq[2][2], rk[2][2];
  f32x4 mR[3];

  auto p1_loop = [&](auto H_) {
    constexpr bool H = H_.value;
    u32x4 Xf[2][3], Wf[2][3], Sf[2];
    f32x4 sA = z4, aC = z4;
#pragma unroll
    for (int i = 0; i < 2; ++i) aQ[i] = z4;
#pragma unroll
    for (int i = 0; i < 3; ++i) { aK[i] = z4; aV[i] = z4; }
    const u32 xl = L_X + (r16 >> 2) * 4096 + (r16 & 3) * 256 + ((q ^ r16) << 4);
    u32 xb[4], xst[4], wr = trN ^ (hh << 5), wr2 = wr + 32768;
#pragma unroll
    for (int i = 0; i < 4; ++i) { xb[i] = (xl ^ (i << 6)) + (H ? 0 : 32768); PINV(xb[i]); }
    PINV(wr); PINV(wr2);
    u32 wov = (hh * 8 + (lane >> 3)) * 1024 + swzW;
    const u32 stok = min(10 * w + 3 * qp + pp, 79u), srow = pp == 3 ? 0u : stok;
    u32 fin = L_MR + stok * 4;
#pragma unroll
    for (int i = 0; i < 4; ++i) {
      xst[i] = L_X + (srow >> 2) * 4096 + (srow & 3) * 256 + (((q + 4 * i) ^ (srow & 15)) << 4);
      PINV(xst[i]);
    }
    PINV(fin);
    if constexpr (!H) PINV(wov);
    u32x2 wh;
    float snm, srv;
    auto rd = [&](auto HS_, auto N_) {
      constexpr int hs = HS_.value, s = hs / 2, kk = hs & 1, n = N_.value;
      constexpr u32 so = L_RING + ((s + 1) % 3) * 24576 + kk * 12288, xo = (hs >> 2) * 1024;
      if constexpr (n == 0) Xf[kk][1] = LD(u32x4, xb[hs & 3] + xo + 16384);
      if constexpr (n >= 1 && n <= 6) {
        constexpr int seg = (n - 1) / 2;
        constexpr u32 o = so + seg * 4096 + ((n - 1) & 1) * 512;
        const u32x2 v = o < 65536 ? trd(wr + o) : trd(wr2 + (o - 32768));
        if constexpr (n & 1) wh = v;
        else Wf[kk][seg] = u32x4{wh.x, wh.y, v.x, v.y};
      }
      if constexpr (n == 7) Xf[kk][2] = LD(u32x4, xb[hs & 3] + xo + 32768);
      if constexpr (n == 8) Xf[kk][0] = LD(u32x4, xb[hs & 3] + xo);
    };
    auto mm = [&](auto KK_, auto N_) {
      constexpr int kk = KK_.value, n = N_.value;
      if constexpr (n == 0) aQ[0] = mfma32(Wf[kk][0], Xf[kk][1], aQ[0]);
      if constexpr (n == 1) aK[1] = mfma32(Wf[kk][1], Xf[kk][1], aK[1]);
      if constexpr (n == 2) aV[1] = mfma32(Xf[kk][1], Wf[kk][2], aV[1]);
      if constexpr (n == 3) aQ[1] = mfma32(Wf[kk][0], Xf[kk][2], aQ[1]);
      if constexpr (n == 4) aK[2] = mfma32(Wf[kk][1], Xf[kk][2], aK[2]);
      if constexpr (n == 5) aV[2] = mfma32(Xf[kk][2], Wf[kk][2], aV[2]);
      if constexpr (H && n == 6) aK[0] = mfma32(Wf[kk][1], Xf[kk][0], aK[0]);
      if constexpr (H && n == 7) aV[0] = mfma32(Xf[kk][0], Wf[kk][2], aV[0]);
      if constexpr (!H && n == 6) aC = mfma32(Wf[kk][0], ones, aC);
    };
    auto stream = [&](auto B_, auto I_) {
      constexpr int B = B_.value, i = I_.value;
      if constexpr (B <= 5 && i < 6) dma_w(IC(B + 2), I_);
      else if constexpr (B <= 3 && i < ((B & 1) ? 8 : 9)) dma_x(IC(i - 6 + (B & 1) * 3), IC(2 + B / 2));
      else if constexpr (B >= 6 && i < (B == 8 ? 4 : 6)) {
        constexpr int p = (B - 6) * 6 + i;
        dma16c<L_RING + p * 4096, p * 32768>(hh * 1024, wov, rs_wo, c * 128);
      }
    };
    wait_vm<(H ? 6 : 5)>();
    BARRIER();
    TS(1);
    sfor<(H ? 9 : 8)>([&](auto n) { rd(IC(0), n); });
    SCHED();
    sfor<16>([&](auto HS_) {
      constexpr int hs = HS_.value, s = hs / 2, kk = hs & 1;
      constexpr int f0 = stat_f0(hs), f1 = stat_f0(hs + 1);
      sfor<(H ? 8 : 7)>([&](auto N_) {
        constexpr int n = N_.value;
        mm(IC(kk), N_);
        SCHED();
        if constexpr (n < f1 - f0) Sf[n] = LD(u32x4, xst[(f0 + n) & 3] + ((f0 + n) >> 2) * 1024);
        if constexpr (H) {
          if constexpr (hs < 15) {
            rd(IC(hs + 1), N_);
            if constexpr (n == 7) rd(IC(hs + 1), IC(8));
          }
        } else {
          if constexpr (hs < 15) {
            if constexpr (n == 0) rd(IC(hs + 1), IC(0));
            rd(IC(hs + 1), IC(n + 1));
          }
          constexpr int B = (hs + 1) / 2;
          if constexpr (hs == 0) {
            if constexpr (n < 6) stream(IC(0), N_);
            if constexpr (n >= 4) stream(IC(0), IC(n + 2));
          } else if constexpr (hs == 15) {
            stream(IC(B), N_);
          } else {
            if constexpr (n < 6 && !(n & 1)) stream(IC(B), IC((1 - kk) * 3 + n / 2));
            if constexpr (kk == 1 && (n == 1 || n == 5)) stream(IC(B), IC(6 + n / 4));
            if constexpr (kk == 0 && n == 1) stream(IC(B), IC(8));
          }
        }
        if constexpr (hs == 13 && n == 3) xres = LD(u32x4, xra);
        if constexpr (hs == 13 && n == 2) {
          float d0 = sA.x, d1 = sA.y, d2 = sA.z;
          PINV(d0); PINV(d1); PINV(d2);
          snm = mul1(sA.w, -1.0f / D);
          srv = mul1(pp == 0 ? d0 : pp == 1 ? d1 : d2, 1.0f / D);
        }
        if constexpr (hs == 13 && n == 4) srv = fmaxf(srv - snm * snm, 0.0f) + EPS;
        if constexpr (hs == 13 && n == 5) srv = __builtin_amdgcn_rsqf(srv);
        if constexpr (hs == 14 && n == 0) {
          if (q == qp && pp != 3) { LD(float, fin) = snm; LD(float, fin + 384) = srv; }
        }
        if constexpr (hs == 15) {
          if constexpr (n == 0) {
#pragma unroll
            for (int a = 0; a < 2; ++a) {
              fq[a][0] = LD(float, mrL + 384 + (a + 1) * 64);
              rk[a][0] = LD(float, rk0a + a * 64);
              rk[a][1] = LD(float, rk1a + a * 64);
            }
          }
          if constexpr (n == 1) {
#pragma unroll
            for (int a = 0; a < 3; ++a) { mL[a] = LD(float, mrL + a * 64); mR[a] = LD(f32x4, mrR + a * 64); }
          }
          if constexpr (n == 4) {
#pragma unroll
            for (int a = 0; a < 2; ++a) {
              const float cq = mul1(fq[a][0], 0.25f * 1.44269504088896f);
              fq[a][0] = mul1(rk[a][0], cq);
              fq[a][1] = mul1(rk[a][1], cq);
            }
          }
        }
        SCHED();
      });
      sfor<f1 - f0>([&](auto I_) {
        sA = mfma32(Sf[I_.value], Sf[I_.value], sA);
        SCHED();
      });
      if constexpr (kk == 0) {
        constexpr int B = s + 1;
        if constexpr (B <= 7) TD(2 * B - 1);
        if constexpr (H) {
          if constexpr (B == 1) wait_vm<0>();
        } else {
          if constexpr (B >= 2 && B < 8) wait_vm<(B == 2 ? 8 : B == 3 ? 9 : B == 4 ? 8 : 6)>();
        }
        barrier_lgkm<(hs == 14 ? 0 : H ? 2 : 1)>();
        if constexpr (B <= 7) TD(2 * B);
        SCHED();
      }
    });
    if constexpr (H) {
      LD(f32x4, exa) = aK[2];
      LD(f32x4, exa + 1024) = aV[2];
      if (r16 == 0) LD(f32x4, csa + 64) = aK[0];
      if (q == 0) LD(float, csv) = aV[0].x;
    } else {
      if (r16 == 0) LD(f32x4, csa) = aC;
    }
    BARRIER();
    if constexpr (!H) {
      aK[0] = LD(f32x4, exa);
      aV[0] = LD(f32x4, exa + 1024);
    }
  };
  if (heavy) p1_loop(std::true_type{}); else p1_loop(std::false_type{});
  TS(2);
  constexpr int NREG = NPRE2;
  u32x4 B1[8][4], B2[7][4], Bo[4][4];
  u32x4 hreg;
  auto bo_read = [&](int k) {
#pragma unroll
    for (int n = 0; n < 4; ++n) Bo[k][n] = trfrag(trO[n] + k * 4096);
  };
  float bias;
  u32x4 bb;
  {
    const f32x4 csQ = LD(f32x4, csa), csK = LD(f32x4, csa + 64);
    const float csV = LD(float, csv);
    bias = blo((u32)LD(u16, L_B2 + s_ * 2));
    bb = LD(u32x4, L_B1 + (w & 1) * 64 + q * 16);
    w1_piece(IC(0), IC(0), IC(SA));
    u32x2 qf[2], kf[3], vf[3];
#pragma unroll
    for (int a = 0; a < 2; ++a) {
      const f32x4 v = aQ[a] + mL[a + 1] * csQ;
      qf[a] = u32x2{pk(v[0], v[1]), pk(v[2], v[3])};
    }
    w1_piece(IC(0), IC(1), IC(SA));
#pragma unroll
    for (int a = 0; a < 3; ++a) {
      const f32x4 v = aK[a] + mL[a] * csK;
      kf[a] = u32x2{pk(v[0], v[1]), pk(v[2], v[3])};
    }
    w1_piece(IC(0), IC(2), IC(SA));
#pragma unroll
    for (int a = 0; a < 3; ++a) {
      const f32x4 v = aV[a] + mR[a] * csV;
      vf[a] = u32x2{pk(v[0], v[1]), pk(v[2], v[3])};
    }
    SCHED();
    f32x4 sp[2], so[2];
#pragma unroll
    for (int a = 0; a < 2; ++a) {
      sp[a] = mfma16(kf[a], qf[a], z4);
      so[a] = mfma16(kf[a + 1], qf[a], z4);
    }
    w1_piece(IC(0), IC(3), IC(SA));
    wait_vm<4>();
    BARRIER();
    bo_read(0);
    SCHED();
    float lg[2][2], mx[2];
#pragma unroll
    for (int a = 0; a < 2; ++a) {
      const float ev0 = own0 ? so[a].x : sp[a].x, od0 = own0 ? so[a].y : sp[a].y;
      const float ev1 = own1 ? so[a].z : sp[a].z, od1 = own1 ? so[a].w : sp[a].w;
      lg[a][0] = (odd ? od0 : ev0) * fq[a][0];
      lg[a][1] = (odd ? od1 : ev1) * fq[a][1];
      if (a == 0) {
        const bool first = (t == 0) && heavy;
        lg[a][0] = (first && !own0) ? -1.44269504088896e9f : lg[a][0];
        lg[a][1] = (first && !own1) ? -1.44269504088896e9f : lg[a][1];
      }
      mx[a] = vmax(lg[a][0], lg[a][1]);
    }
    w1_piece(IC(1), IC(0), IC(SB));
    bo_read(1);
    SCHED();
    allred2<true>(mx[0], mx[1]);
    float ee[2][2], sm[2];
#pragma unroll
    for (int a = 0; a < 2; ++a) {
      ee[a][0] = __builtin_amdgcn_exp2f(lg[a][0] - mx[a]);
      ee[a][1] = __builtin_amdgcn_exp2f(lg[a][1] - mx[a]);
      sm[a] = ee[a][0] + ee[a][1];
    }
    w1_piece(IC(1), IC(1), IC(SB));
    bo_read(2);
    SCHED();
    allred2<false>(sm[0], sm[1]);
    u32x4 pf[2];
#pragma unroll
    for (int a = 0; a < 2; ++a) {
      const float inv = __builtin_amdgcn_rcpf(sm[a]);
      const u32 pw = pk(ee[a][0] * rk[a][0] * inv, ee[a][1] * rk[a][1] * inv);
      const u32 p0 = __builtin_amdgcn_perm(0u, pw, sel0), p1 = __builtin_amdgcn_perm(0u, pw, sel1);
      pf[a] = u32x4{own0 ? 0u : p0, own1 ? 0u : p1, own0 ? p0 : 0u, own1 ? p1 : 0u};
      if (a == 0) w1_piece(IC(1), IC(2), IC(SB));
    }
    f32x4 o[2];
#pragma unroll
    for (int a = 0; a < 2; ++a) o[a] = mfma32(u32x4{vf[a].x, vf[a].y, vf[a + 1].x, vf[a + 1].y}, pf[a], z4);
    w1_piece(IC(1), IC(3), IC(SB));
    auto s0 = __builtin_amdgcn_permlane16_swap(pk(o[0][0], o[0][1]), pk(o[1][0], o[1][1]), false, false);
    auto s1 = __builtin_amdgcn_permlane16_swap(pk(o[0][2], o[0][3]), pk(o[1][2], o[1][3]), false, false);
    const u32x4 ov = {s0[0], s1[0], s0[1], s1[1]};
    st_sc1(ov, rs_ws, ooff, WS_O + (t * 8 + c1) * 8192 + ta * 4096 + hh * 512);
  }
  TS(3);
  bo_read(3);
  w1_issue(IC(2), IC(SC));
  u32 trS[4], lane16 = lane * 16;
#pragma unroll
  for (int n = 0; n < 4; ++n) { trS[n] = (trN ^ (n << 5)) + stg + SA; PINV(trS[n]); }
  auto rd4 = [&](u32x4(&B)[4], u32 slot) {
#pragma unroll
    for (int n = 0; n < 4; ++n) B[n] = trfrag(trS[n] + (slot - SA));
  };
  auto lgkm0 = [&]() { SCHED(); WAIT_LGKM0(); SCHED(); };
  u32 w2E = (lane >> 3) * (S * 2) + swzE, w2O = (lane >> 3) * (S * 2) + (swzE ^ 64) + 8 * (S * 2) - 1024;
  const u32 w2s = w * 224 * (S * 2) + t * 128;
  auto w2_issue = [&](auto K_, auto LC_) { dma16x4<8 * S * 2, LC_.value>(stg, w2E, w2O, rs_w2, w2s + K_.value * 32 * (S * 2)); };
  u32 redw = q * 256 + ((r16 ^ ((q >> 1) << 1)) << 4);
  u32 redr = (((gq >> 1) * 4 + (s_ >> 4)) * 1024) + ((gq & 1) * 32 + ((s_ & 15) ^ (gq << 1))) * 16;
  PINV(redw); PINV(redr); PINV(lane16);
  wait_vm<4>();
  BARRIER();
  if (w == 0 && lane < 8) put8(ws + (WS_F1 + t * 1024 + c1 * 8), lane * 128, tag);
  TS(4);
  {
    rd4(B1[0], SA); rd4(B1[1], SB);
    lgkm0();
    w1_issue(IC(3), IC(SA));
    poll_tag<2>(ws + (WS_F1 + g * 128 + hh * 16), lane, tag);
    TS(5);
    u32x4 ofr[8];
    {
      const u32 os0 = WS_O + (t * 8 + 2 * hh) * 8192 + th * 4096, os1 = os0 + 8192;
      asm volatile(
          "buffer_load_dwordx4 %0, %8, %9, %10 offen sc1\n\t"
          "buffer_load_dwordx4 %1, %8, %9, %10 offen offset:1024 sc1\n\t"
          "buffer_load_dwordx4 %2, %8, %9, %11 offen sc1\n\t"
          "buffer_load_dwordx4 %3, %8, %9, %11 offen offset:1024 sc1\n\t"
          "buffer_load_dwordx4 %4, %8, %9, %10 offen offset:2048 sc1\n\t"
          "buffer_load_dwordx4 %5, %8, %9, %10 offen offset:3072 sc1\n\t"
          "buffer_load_dwordx4 %6, %8, %9, %11 offen offset:2048 sc1\n\t"
          "buffer_load_dwordx4 %7, %8, %9, %11 offen offset:3072 sc1"
          : "=&v"(ofr[0]), "=&v"(ofr[1]), "=&v"(ofr[2]), "=&v"(ofr[3]), "=&v"(ofr[4]), "=&v"(ofr[5]), "=&v"(ofr[6]), "=&v"(ofr[7])
          : "v"(lane16), "s"(rs_ws), "s"(os0), "s"(os1)
          : "memory");
    }
    rd4(B1[2], SC); rd4(B1[3], SA);
    SCHED();
    sfor<2>([&](auto TT_) {
      constexpr int tt = TT_.value;
      f32x4 acc[4];
#pragma unroll
      for (int n = 0; n < 4; ++n) acc[n] = z4;
      sfor<4>([&](auto K_) {
        constexpr int k = K_.value, i = tt * 4 + k;
        wait_tie<7 - i + i / 2>(ofr[i]);
#pragma unroll
        for (int n = 0; n < 4; ++n) acc[n] = mfma32(Bo[k][n], ofr[i], acc[n]);
        if constexpr (i & 1) w1_piece(IC(4), IC(i / 2), IC(SB));
        SCHED();
      });
#pragma unroll
      for (int n = 0; n < 4; ++n) LD(f32x4, L_RING + hh * 16384 + th * 2048 + ((redw ^ (n << 6)) + (n * 4 + tt) * 1024)) = acc[n];
    });
    TS(19);
    BARRIER();
    TS(6);
    f32x4 a0 = LD(f32x4, L_RING + redr), a1 = LD(f32x4, L_RING + redr + 256);
#pragma unroll
    for (int k = 1; k < 4; ++k) {
      a0 += LD(f32x4, L_RING + k * 16384 + redr);
      a1 += LD(f32x4, L_RING + k * 16384 + redr + 256);
    }
    hreg = u32x4{pk(blo(xres.x) + a0[0], bhi(xres.x) + a0[1]), pk(blo(xres.y) + a0[2], bhi(xres.y) + a0[3]),
                 pk(blo(xres.z) + a1[0], bhi(xres.z) + a1[1]), pk(blo(xres.w) + a1[2], bhi(xres.w) + a1[3])};
    const f32x4 h0 = {blo(hreg.x), bhi(hreg.x), blo(hreg.y), bhi(hreg.y)}, h1 = {blo(hreg.z), bhi(hreg.z), blo(hreg.w), bhi(hreg.w)};
    const f32x4 hs = h0 + h1, hq = h0 * h0 + h1 * h1;
    const float ps = sum8((hs.x + hs.y) + (hs.z + hs.w)), pq = sum8((hq.x + hq.y) + (hq.z + hq.w));
    if (gq == 0) st8_sc1(u32x2{min(f2u(ps), 0xfffffffeu), min(f2u(pq), 0xfffffffeu)}, rs_ws, s_ * 8, WS_ST + g * 512);
    TS(7);
    w1_issue(IC(5), IC(SC));
    if constexpr (S3N == 2) w1_issue(IC(6), IC(SA));
    if constexpr (SDELAY > 0) __builtin_amdgcn_s_sleep(SDELAY);
    u32x2 sv;
    for (int sp = 0; sp < SPIN_CAP; ++sp) {
      FENCE();
      sv = __builtin_amdgcn_raw_buffer_load_b64(rs_ws, gq * 512 + s_ * 8, WS_ST + t * 4096, 16);
      if (!__any(max(sv.x, sv.y) == 0xffffffffu)) break;
    }
    TS(8);
    rd4(B1[5], SC);
    {
      const float m = sum8(u2f(sv.x)) * (1.0f / D);
      const float v = fmaxf(sum8(u2f(sv.y)) * (1.0f / D) - m * m, 0.0f);
      const float r1 = __builtin_amdgcn_rsqf(v + EPS);
      const float r2 = __builtin_amdgcn_rsqf(v * r1 * r1 + EPS);
      const float r = r1 * r2;
      const f32x4 n0 = (h0 - m) * r, n1 = (h1 - m) * r;
      const u32x4 hn = {pk(n0[0], n0[1]), pk(n0[2], n0[3]), pk(n1[0], n1[1]), pk(n1[2], n1[3])};
      const u32 tb = SC + w * 4096;
      LD(u32x4, tb + (lane16 ^ ((q & 1) << 6))) = hn;
      FENCE();
      const u32x4 hf = trfrag(tb + qp * 128 + ((q * 32) ^ ((qp >> 1) << 6)) + pp * 8);
      st_sc1(hf, rs_ws, q * 1024 + r16 * 16, WS_HN + c * 262144 + (2 * t + th) * 4096 + hh * 256);
    }
    SCHED();
    w1_issue(IC(7), IC(SC));
    rd4(B1[4], SB);
    if constexpr (S3N == 2) rd4(B1[6], SA);
    wait_vm<4>();
    BARRIER();
    if (w == 0 && lane < 32) put8(ws + (WS_F2 + c * 256 + t * 8), lane * 2048, tag);
    TS(9);
  }

  if constexpr (S3N == 1) w1_issue(IC(6), IC(32768));
  w2_issue(IC(NREG), IC(SB));
  if constexpr (NREG >= 1) w2_issue(IC(0), IC(SA));
  if constexpr (NREG >= 2) w2_issue(IC(1), IC(0));
  if (p3) {
    const u32 mte = w >> 1, kp = w & 1;
    poll_tag<4>(ws + (WS_F2 + g * 256 + w * 32), lane, tag);
    TS(10);
    constexpr int RING = RING3;
    u32x4 A[RING];
    const u32 as = WS_HN + c * 262144 + w * 32768;
#pragma unroll
    for (int i = 0; i < RING; ++i) A[i] = ld_sc1(rs_ws, lane16 + (i >> 3) * 1024, as + (i & 7) * 4096);
    SCHED();
    rd4(B1[7], SC);
    if constexpr (S3N == 1) rd4(B1[6], 32768);
    if constexpr (NREG >= 1) rd4(B2[0], SA);
    if constexpr (NREG >= 2) rd4(B2[1], 0);
    BARRIER();
    SCHED();
#pragma unroll
    for (int mt = 0; mt < 4; ++mt) {
      if (mt == 0) __builtin_amdgcn_s_setprio(3);
      if (mt == 1) __builtin_amdgcn_s_setprio(2);
      if (mt == 2) __builtin_amdgcn_s_setprio(1);
      if (mt == 3) __builtin_amdgcn_s_setprio(0);
      f32x4 acc[4];
#pragma unroll
      for (int n = 0; n < 4; ++n) acc[n] = z4;
#pragma unroll
      for (int k = 0; k < 8; ++k) {
        const int i = mt * 8 + k;
        const u32x4 cur = A[i % RING];
#pragma unroll
        for (int n = 0; n < 4; ++n) acc[n] = mfma32(B1[k][n], cur, acc[n]);
        if (i + RING < 32) A[i % RING] = ld_sc1(rs_ws, lane16 + ((i + RING) >> 3) * 1024, as + ((i + RING) & 7) * 4096);
        if (i == 27) rd4(B2[NREG], SB);
        SCHED();
      }
      if (mt == 3) {
        WAIT_LGKM0();
        w2_issue(IC(NREG + 1), IC(SB));
      }
#pragma unroll
      for (int n = 0; n < 4; ++n) LD(f32x4, w * 16384 + lane16 + (mt * 4 + n) * 1024) = acc[n];
    }
    TS(16);
    BARRIER();
    TS(11);
    if (j == 0 && c == 0 && tid == 0) *(GAS u64*)(ws + WS_EPOCH) = epoch + 1;
    {
      f32x4 s0 = {blo(bb.x), bhi(bb.x), blo(bb.y), bhi(bb.y)}, s1 = {blo(bb.z), bhi(bb.z), blo(bb.w), bhi(bb.w)};
      const u32 ra = (mte * 4 + 2 * kp + (q >> 1)) * 1024 + ((q & 1) * 32 + r16) * 16;
#pragma unroll
      for (int ww = 0; ww < 8; ++ww) {
        s0 += LD(f32x4, ww * 16384 + ra);
        s1 += LD(f32x4, ww * 16384 + ra + 256);
      }
      float v[8] = {s0[0], s0[1], s0[2], s0[3], s1[0], s1[1], s1[2], s1[3]};
#pragma unroll
      for (int e = 0; e < 8; ++e) {
        const float z = v[e] * (v[e] * v[e] * (-0.044715f * 2.302208198f) - 2.302208198f);
        v[e] = v[e] * __builtin_amdgcn_rcpf(1.0f + __builtin_amdgcn_exp2f(z));
      }
      const u32x4 uf = {pk(v[0], v[1]), pk(v[2], v[3]), pk(v[4], v[5]), pk(v[6], v[7])};
      st_sc1(uf, rs_ws, lane16, WS_U + c * (56 * 4096) + (2 * j + kp) * 4096 + mte * 1024);
    }
    BARRIER();
    w2_issue(IC(NREG + 2), IC(SA));
    wait_vm<4>();
    BARRIER();
    if (w == 0 && lane < 32) put8(ws + (WS_F3 + c * 256 + j * 8), lane * 2048, tag);
  } else {
    wait_vm<0>();
    if constexpr (NREG >= 1) rd4(B2[0], SA);
    if constexpr (NREG >= 2) rd4(B2[1], 0);
    rd4(B2[NREG], SB);
    lgkm0();
    w2_issue(IC(NREG + 1), IC(SB));
    w2_issue(IC(NREG + 2), IC(SA));
    sfor<4 - NREG>([&](auto i) { w2_issue(IC(NREG + 3 + i.value), IC(i.value * 32768)); });
  }
  TS(12);

  {
    constexpr int NR = 4 - NREG;
    if (p3) sfor<NR>([&](auto i) { w2_issue(IC(NREG + 3 + i.value), IC(i.value * 32768)); });
    TS(13);
    poll_tag<4>(ws + (WS_F3 + g * 256 + ((7 * w) >> 1) * 8), lane, tag);
    TS(14);
    constexpr int RING = RING4;
    u32x4 A[RING];
    const u32 as = WS_U + c * (56 * 4096) + w * (7 * 4096);
#pragma unroll
    for (int i = 0; i < RING; ++i) A[i] = ld_sc1(rs_ws, lane16 + (i / 7) * 1024, as + (i % 7) * 4096);
    SCHED();
    rd4(B2[NREG + 1], SB);
    rd4(B2[NREG + 2], SA);
    sfor<NR>([&](auto i) { rd4(B2[NREG + 3 + i.value], i.value * 32768); });
    BARRIER();
    SCHED();
#pragma unroll
    for (int mt = 0; mt < 4; ++mt) {
      if (mt == 0) __builtin_amdgcn_s_setprio(3);
      if (mt == 1) __builtin_amdgcn_s_setprio(2);
      if (mt == 2) __builtin_amdgcn_s_setprio(1);
      if (mt == 3) __builtin_amdgcn_s_setprio(0);
      f32x4 acc[4];
#pragma unroll
      for (int n = 0; n < 4; ++n) acc[n] = z4;
#pragma unroll
      for (int k = 0; k < 7; ++k) {
        const int i = mt * 7 + k;
        const u32x4 cur = A[i % RING];
#pragma unroll
        for (int n = 0; n < 4; ++n) acc[n] = mfma32(cur, B2[k][n], acc[n]);
        if (i + RING < 28) A[i % RING] = ld_sc1(rs_ws, lane16 + ((i + RING) / 7) * 1024, as + ((i + RING) % 7) * 4096);
        SCHED();
      }
#pragma unroll
      for (int n = 0; n < 4; ++n) LD(f32x4, w * 16384 + (redw ^ (mt << 6)) + (mt * 4 + n) * 1024) = acc[n];
    }
    f32x4 s0 = {blo(hreg.x) + bias, bhi(hreg.x) + bias, blo(hreg.y) + bias, bhi(hreg.y) + bias};
    f32x4 s1 = {blo(hreg.z) + bias, bhi(hreg.z) + bias, blo(hreg.w) + bias, bhi(hreg.w) + bias};
    TS(17);
    BARRIER();
    TS(18);
#pragma unroll
    for (int ww = 0; ww < 8; ++ww) {
      s0 += LD(f32x4, ww * 16384 + redr);
      s1 += LD(f32x4, ww * 16384 + redr + 256);
    }
    const u32x4 ov = {pk(s0[0], s0[1]), pk(s0[2], s0[3]), pk(s1[0], s1[1]), pk(s1[2], s1[3])};
    st_sc1(ov, rs_out, s_ * 1024 + gq * 16, t * 65536 + c * 128);
  }
  if (probe_t) while (__builtin_amdgcn_s_memrealtime() - probe_t < 4000) __builtin_amdgcn_s_sleep(1);
  asm volatile(".Lfb_end_mark:" ::: "memory");
}

extern "C" void kernel_launch(void* const* d_in, const int* in_sizes, int n_in, void* d_out, int out_size, void* d_ws, size_t ws_size,
                              hipStream_t stream) {
  (void)in_sizes; (void)n_in; (void)out_size; (void)ws_size;
  (void)hipFuncSetAttribute((const void*)fused_block, hipFuncAttributeMaxDynamicSharedMemorySize, L_TOTAL);
  fused_block<<<256, 512, L_TOTAL, stream>>>((const u16*)d_in[0], (const u16*)d_in[1], (char*)d_ws, (const u16*)d_in[2], (const u16*)d_in[4],
                                             (const u16*)d_in[6], (const u16*)d_in[5], (const u16*)d_in[7], (u16*)d_out);
}
```

```cpp
#include <hip/hip_runtime.h>
#include <utility>
#pragma clang diagnostic ignored "-Wint-to-pointer-cast"

typedef unsigned short u16;
typedef unsigned u32;
typedef unsigned long long u64;
typedef __bf16 bf16x8 __attribute__((ext_vector_type(8)));
typedef __bf16 bf16x2 __attribute__((ext_vector_type(2)));
typedef short s16x4 __attribute__((ext_vector_type(4)));
typedef float f32x4 __attribute__((ext_vector_type(4)));
typedef float f32x2 __attribute__((ext_vector_type(2)));
typedef u32 u32x4 __attribute__((ext_vector_type(4)));
typedef u32 u32x2 __attribute__((ext_vector_type(2)));
#define LAS __attribute__((address_space(3)))
#define GAS __attribute__((address_space(1)))
#define DI __device__ __forceinline__
typedef __amdgpu_buffer_rsrc_t rsrc_t;

constexpr int S = 2048, D = 512, F = 1792;
constexpr float EPS = 1e-5f;
constexpr u32 WS_EPOCH = 0;
constexpr u32 WS_F1 = 4096;
constexpr u32 WS_F2 = WS_F1 + 256 * 128;
constexpr u32 WS_F3 = WS_F2 + 256 * 256;
constexpr u32 WS_ST = WS_F3 + 256 * 256;
constexpr u32 WS_O = 2u << 20;
constexpr u32 WS_HN = WS_O + (2u << 20);
constexpr u32 WS_U = WS_HN + (2u << 20);
constexpr u32 WS_END = WS_U + (2u << 20);
constexpr u32 L_RING = 0;
constexpr u32 L_X = 81920;
constexpr u32 L_MISC = 73728;
constexpr u32 L_CS = L_MISC + 4096;
constexpr u32 L_MR = L_MISC + 5120;
constexpr u32 L_SCR = L_MISC + 6144;
constexpr u32 L_B1 = L_MISC + 7680;
constexpr u32 L_B2 = L_MISC + 7936;
constexpr u32 L_TOTAL = 163840;
constexpr u32 OOB = 0x7ffffff0u;
constexpr int NREP = 16;
constexpr int SPIN_CAP = 1 << 18;

DI void dma4(u32 lds, u32 voff, rsrc_t rs, u32 soff) {
  asm volatile("s_mov_b32 m0, %0\n\ts_nop 0\n\tbuffer_load_dword %1, %2, %3 offen lds" ::"s"(lds), "v"(voff), "s"(rs), "s"(soff) : "memory");
}
template <int LC, int SC>
DI void dma16c(u32 lds, u32 voff, rsrc_t rs, u32 soff) {
  u32 tmp;
  asm volatile("s_add_u32 m0, %1, %5\n\ts_add_u32 %0, %4, %6\n\tbuffer_load_dwordx4 %2, %3, %0 offen lds"
               : "=&s"(tmp) : "s"(lds), "v"(voff), "s"(rs), "s"(soff), "n"(LC), "n"(SC) : "memory", "scc");
}
template <int LC, int SC>
DI void dma16m(u32 lds, u32 voff, rsrc_t rs, u32 soff, u64 mask) {
  u32 tmp;
  asm volatile("s_add_u32 m0, %1, %5\n\ts_add_u32 %0, %4, %6\n\ts_mov_b64 exec, %7\n\tbuffer_load_dwordx4 %2, %3, %0 offen lds\n\ts_mov_b64 exec, -1"
               : "=&s"(tmp) : "s"(lds), "v"(voff), "s"(rs), "s"(soff), "n"(LC), "n"(SC), "s"(mask) : "memory", "scc");
}
template <int STEP, int LC>
DI void dma16x4(u32 lds, u32 voffE, u32 voffO, rsrc_t rs, u32 soff) {
  u32 tmp;
  asm volatile(
      "s_add_u32 m0, %1, %7\n\t"
      "s_add_u32 %0, %5, %6\n\t"
      "buffer_load_dwordx4 %2, %4, %5 offen lds\n\t"
      "buffer_load_dwordx4 %3, %4, %5 offen offset:1024 lds\n\t"
      "buffer_load_dwordx4 %2, %4, %0 offen offset:2048 lds\n\t"
      "buffer_load_dwordx4 %3, %4, %0 offen offset:3072 lds"
      : "=&s"(tmp)
      : "s"(lds), "v"(voffE), "v"(voffO), "s"(rs), "s"(soff), "n"(2 * STEP - 2048), "n"(LC)
      : "memory", "scc");
}
template <int N> DI void wait_vm() { asm volatile("s_waitcnt vmcnt(%0)" ::"n"(N) : "memory"); }
template <int N> DI void wait_tie(u32x4& v) { asm volatile("s_waitcnt vmcnt(%1)" : "+v"(v) : "n"(N) : "memory"); }
template <int N> DI void barrier_lgkm() { asm volatile("s_waitcnt lgkmcnt(%0)\n\ts_barrier" ::"n"(N) : "memory"); }
#define WAIT_LGKM0() asm volatile("s_waitcnt lgkmcnt(0)" ::: "memory")
#define BARRIER() barrier_lgkm<0>()
#define SCHED() __builtin_amdgcn_sched_barrier(0)
#define FENCE() asm volatile("" ::: "memory")
#define PINV(x) asm volatile("" : "+v"(x))
#define PINS(x) asm volatile("" : "+s"(x))
#define IC(n) std::integral_constant<int, (n)>{}
#define LD(T, a) (*(LAS T*)(a))

DI f32x4 mfma32(u32x4 a, u32x4 b, f32x4 c) {
  return __builtin_amdgcn_mfma_f32_16x16x32_bf16(__builtin_bit_cast(bf16x8, a), __builtin_bit_cast(bf16x8, b), c, 0, 0, 0);
}
DI f32x4 mfma16(u32x2 a, u32x2 b, f32x4 c) {
  return __builtin_amdgcn_mfma_f32_16x16x16bf16_1k(__builtin_bit_cast(s16x4, a), __builtin_bit_cast(s16x4, b), c, 0, 0, 0);
}
DI u32x2 trd(u32 addr) { return __builtin_bit_cast(u32x2, __builtin_amdgcn_ds_read_tr16_b64_v4i16((LAS s16x4*)(addr))); }
DI u32x4 trfrag(u32 a) {
  u32x2 lo = trd(a), hi = trd(a + 512);
  return u32x4{lo.x, lo.y, hi.x, hi.y};
}
DI u32 pk(float a, float b) {
  f32x2 v = {a, b};
  return __builtin_bit_cast(u32, __builtin_convertvector(v, bf16x2));
}
DI float blo(u32 v) { return __builtin_bit_cast(float, v << 16); }
DI float bhi(u32 v) { return __builtin_bit_cast(float, v & 0xffff0000u); }
DI u32 f2u(float f) { return __builtin_bit_cast(u32, f); }
DI float u2f(u32 u) { return __builtin_bit_cast(float, u); }
DI float vmax(float a, float b) { return __builtin_elementwise_maximum(a, b); }
DI float mul1(float a, float b) { float r = a * b; PINV(r); return r; }
template <bool MAX>
DI void allred2(float& a, float& b) {
  auto r1 = __builtin_amdgcn_permlane16_swap(f2u(a), f2u(b), false, false);
  float t = MAX ? vmax(u2f(r1[0]), u2f(r1[1])) : u2f(r1[0]) + u2f(r1[1]);
  auto r2 = __builtin_amdgcn_permlane32_swap(f2u(t), f2u(t), false, false);
  float u = MAX ? vmax(u2f(r2[0]), u2f(r2[1])) : u2f(r2[0]) + u2f(r2[1]);
  auto r3 = __builtin_amdgcn_permlane16_swap(f2u(u), f2u(u), false, false);
  a = u2f(r3[0]);
  b = u2f(r3[1]);
}
template <int CTRL> DI float dppadd(float v) { float r = v + __builtin_amdgcn_update_dpp(0.0f, v, CTRL, 0xf, 0xf, true); PINV(r); return r; }
DI float sum8(float v) {
  v = dppadd<0xB1>(v);
  v = dppadd<0x4E>(v);
  v = dppadd<0x141>(v);
  return v;
}
DI u32x4 ld_sc1(rsrc_t rs, u32 voff, u32 soff) { return __builtin_amdgcn_raw_buffer_load_b128(rs, voff, soff, 16); }
DI void st_sc1(u32x4 v, rsrc_t rs, u32 voff, u32 soff) {
  __builtin_amdgcn_raw_buffer_store_b128(v, rs, voff, soff, 16);
  asm volatile("s_nop 1" ::"v"(v) : "memory");
}
DI void st8_sc1(u32x2 v, rsrc_t rs, u32 voff, u32 soff) { __builtin_amdgcn_raw_buffer_store_b64(v, rs, voff, soff, 16); }

DI void put8(char* base, u32 off, u64 v) { (void)__hip_atomic_exchange((GAS u64*)(base + off), v, __ATOMIC_RELAXED, __HIP_MEMORY_SCOPE_AGENT); }
DI u64 get8(char* base, u32 off) {
  u64 zero = 0;
  PINV(zero);
  return __hip_atomic_fetch_add((GAS u64*)(base + off), zero, __ATOMIC_RELAXED, __HIP_MEMORY_SCOPE_AGENT);
}
template <int N>
DI void poll_tag(char* base, u32 lane, u64 tag) {
  if (lane < N) {
    for (int sp = 0; sp < SPIN_CAP; ++sp) {
      if (__all(get8(base, lane * 8) == tag)) break;
    }
  }
  FENCE();
}
DI float dot2(u32 a, u32 b, float c) {
  return __builtin_amdgcn_fdot2_f32_bf16(__builtin_bit_cast(bf16x2, a), __builtin_bit_cast(bf16x2, b), c, false);
}
template <typename Fn>
DI void red16(u32 a, f32x4& s0, f32x4& s1, Fn&& between) {
  f32x4 p[16];
#pragma unroll
  for (int i = 0; i < 8; ++i) {
    p[2 * i] = LD(f32x4, a + i * 4096);
    p[2 * i + 1] = LD(f32x4, a + i * 4096 + 256);
  }
  SCHED();
  between();
  SCHED();
  s0 += ((p[0] + p[2]) + (p[4] + p[6])) + ((p[8] + p[10]) + (p[12] + p[14]));
  s1 += ((p[1] + p[3]) + (p[5] + p[7])) + ((p[9] + p[11]) + (p[13] + p[15]));
}
template <typename Fn, int... I> DI void sfor_impl(Fn&& f, std::integer_sequence<int, I...>) { (f(std::integral_constant<int, I>{}), ...); }
template <int N, typename Fn> DI void sfor(Fn&& f) { sfor_impl(f, std::make_integer_sequence<int, N>{}); }
constexpr int stat_f0(int hs) {
  constexpr int v[14] = {0, 1, 2, 4, 5, 6, 8, 9, 10, 12, 13, 14, 15, 16};
  return v[hs < 13 ? hs : 13];
}

__global__ __launch_bounds__(512, 2) void fused_block(const u16* __restrict__ x, const u16* __restrict__ wqkv, char* __restrict__ ws,
                                                      const u16* __restrict__ wo, const u16* __restrict__ w1, const u16* __restrict__ w2,
                                                      const u16* __restrict__ b1, const u16* __restrict__ b2, u16* __restrict__ out) {
  const u32 tid = threadIdx.x, lane = tid & 63;
  const u32 w = __builtin_amdgcn_readfirstlane(tid >> 6);
  const u32 b = blockIdx.x, xcd = b & 7, idx = b >> 3;
  if (w == 7) {
    u64 pc;
    u32 nbytes;
    asm volatile("s_getpc_b64 %0\n.Lfb_pc_mark:\n\ts_mov_b32 %1, .Lfb_end_mark-.Lfb_pc_mark" : "=s"(pc), "=s"(nbytes));
    const rsrc_t rs_pc = __builtin_amdgcn_make_buffer_rsrc((void*)(pc & ~(u64)15), 0, nbytes, 0x00020000);
    dma16c<L_SCR, 0>(0, lane * 16 + idx * 1024, rs_pc, 0);
  }
  asm volatile("" : "+s"(x), "+s"(wqkv), "+s"(ws), "+s"(wo), "+s"(w1), "+s"(w2), "+s"(b1), "+s"(b2), "+s"(out));
  u64 epoch;
  asm volatile("s_load_dwordx2 %0, %1, 0x0" : "=s"(epoch) : "s"(ws));
  const u32 c = (xcd & 1) * 4 + (idx & 3), tl = idx >> 2, sq = xcd >> 1, t = sq * 8 + tl;
  const u32 hh = w & 3, th = w >> 2;
  const bool heavy = w >= 4;
  const u32 r16 = lane & 15, q = lane >> 4;

  const rsrc_t rs_x = __builtin_amdgcn_make_buffer_rsrc((void*)x, 0, S * D * 2, 0x00020000);
  const rsrc_t rs_wqkv = __builtin_amdgcn_make_buffer_rsrc((void*)wqkv, 0, D * 3 * D * 2, 0x00020000);

  const u32 xrw = 4 * hh + q;
  const u32 xchunk = (r16 ^ xrw) << 4;
  const u32 xv = xrw * 1024 + xchunk;
  const u32 xv0 = t == 0 ? xchunk : xv;
  const u32 xs0 = t == 0 ? 0 : t * 65536 - 16384;
  const u64 xmask = hh == 0 ? ~0xffffull : ~0ull;
  const u32 swzE = ((lane & 7) ^ ((q & 1) << 1)) << 4;
  const u32 swzW = swzE ^ ((hh & 1) << 6);
  const u32 wv = (hh * 8 + (lane >> 3)) * 3072 + swzW;
  auto dma_x = [&](auto A_, auto Q_) {
    constexpr int a = A_.value, qt = Q_.value;
    if constexpr (a == 0) dma16m<L_X + qt * 1024, qt * 256>(hh * 4096, xv0, rs_x, xs0, xmask);
    else dma16c<L_X + a * 16384 + qt * 1024, (a - 1) * 16384 + qt * 256>(hh * 4096, xv, rs_x, t * 65536);
  };
  auto dma_w = [&](auto S_, auto I_) {
    constexpr int s = S_.value, seg = I_.value % 3, hi = I_.value / 3;
    dma16c<L_RING + ((s + 1) % 3) * 24576 + hi * 12288 + seg * 4096, (s * 64 + hi * 32) * 3072 + seg * 1024>(hh * 1024, wv, rs_wqkv, c * 128);
  };
  const u32 cs = (3u - ((u32)(size_t)wqkv >> 7)) & 7u;
  asm volatile("s_nop 4" ::: "memory");
  if (heavy) {
    sfor<6>([&](auto i) { dma_w(IC(0), i); });
    if (w == 4) dma4(L_SCR, lane < 48 ? lane * 128 : OOB, rs_wqkv, b * 6144);
    if (w == 5) dma4(L_SCR, (lane & 7) * 1024 + min(2 + (lane >> 3), 7u) * 128, rs_x, (t * 64 + c * 8) * 1024);
    if (w == 6) dma4(L_SCR, (lane & 15) * 128, __builtin_amdgcn_make_buffer_rsrc((void*)wo, 0, D * D * 2, 0x00020000), b * 2048);
    if (w == 7) dma4(L_SCR, (cs >> 2) == (c >> 2) && cs != c ? lane * 3072 : OOB, rs_wqkv, tl * 64 * 3072 + (((c - cs - 1) & 3) % 3) * 1024 + cs * 128);
  } else {
    sfor<5>([&](auto a) { dma_x(a, IC(0)); });
  }
  const u32 g = t * 8 + c, gr = (t * NREP / 32) * 8 + c;
  const u32 tidle = (3u - ((u32)(size_t)w2 >> 7)) & 7u;
  const bool p3 = tl != tidle;
  const u32 j = sq * 7 + (tl > tidle ? tl - 1 : p3 ? tl : 0);
  const rsrc_t rs_wo = __builtin_amdgcn_make_buffer_rsrc((void*)wo, 0, D * D * 2, 0x00020000);
  const rsrc_t rs_w1 = __builtin_amdgcn_make_buffer_rsrc((void*)w1, 0, p3 ? S * F * 2 : 0, 0x00020000);
  const rsrc_t rs_w1t = __builtin_amdgcn_make_buffer_rsrc((void*)w1, 0, S * F * 2, 0x00020000);
  const rsrc_t rs_w2 = __builtin_amdgcn_make_buffer_rsrc((void*)w2, 0, F * S * 2, 0x00020000);
  const rsrc_t rs_ws = __builtin_amdgcn_make_buffer_rsrc((void*)ws, 0, WS_END, 0x00020000);
  const rsrc_t rs_out = __builtin_amdgcn_make_buffer_rsrc((void*)out, 0, S * D * 2, 0x00020000);
  u32x4 ones = {0x3f803f80u, 0x3f803f80u, 0x3f803f80u, 0x3f803f80u};
  PINV(ones);
  if (w == 5) LD(u32x4, L_X + q * 1024 + r16 * 16) = ones;

  const u32 ta = heavy ? 0u : 1u;
  const u32 qp = r16 >> 2, pp = lane & 3;
  u32 trN = (8 * q + qp) * 128 + ((((qp >> 1) & 1) | ((q & 1) << 1)) << 5) + 8 * pp;
  u32 w1E = (lane >> 3) * (F * 2) + swzE, w1O = (lane >> 3) * (F * 2) + (swzE ^ 64) + 8 * (F * 2) - 1024;
  const u32 k0 = 4 * q + (r16 & 1);
  const bool own0 = k0 <= r16, own1 = k0 + 2 <= r16, odd = r16 & 1;
  u32 mrL = L_MR + ta * 128 + r16 * 4, mrR = L_MR + ta * 128 + q * 16;
  u32 rk0a = L_MR + 384 + ta * 128 + (k0 + (own0 ? 16 : 0)) * 4, rk1a = L_MR + 384 + ta * 128 + (k0 + 2 + (own1 ? 16 : 0)) * 4;
  const u32 se0 = odd ? 0x01000c0cu : 0x0c0c0100u, se1 = odd ? 0x03020c0cu : 0x0c0c0302u;
  u32 sel0 = own0 ? 0x0c0c0c0cu : se0, sel1 = own1 ? 0x0c0c0c0cu : se1, sel2 = own0 ? se0 : 0x0c0c0c0cu, sel3 = own1 ? se1 : 0x0c0c0c0cu;
  PINV(sel2); PINV(sel3);
  u32 csa = L_CS + hh * 192 + q * 16, csv = L_CS + hh * 192 + 128 + r16 * 4;
  u32 exa = L_X + hh * 2048 + lane * 16;
  u32 ooff = (q & 1) * 2048 + ((q >> 1) * 16 + r16) * 16;
  const u32 s_ = tid >> 3, gq = tid & 7;
  u32 xra;
  {
    const u32 row = 16 + s_, ch = c * 8 + gq;
    xra = L_X + (row >> 2) * 4096 + (ch >> 4) * 1024 + (row & 3) * 256 + (((ch & 15) ^ (row & 15)) << 4);
  }
  PINV(trN); PINV(w1E); PINV(w1O); PINV(mrL); PINV(mrR); PINV(rk0a); PINV(rk1a); PINV(sel0); PINV(sel1); PINV(csa); PINV(csv); PINV(exa);
  PINV(ooff); PINV(xra);
  u32 trO[4];
#pragma unroll
  for (int n = 0; n < 4; ++n) { trO[n] = (trN ^ (n << 5)) + hh * 16384; PINV(trO[n]); }
  u32 w1s = w * 256 * (F * 2) + j * 128, stg = w * 4096;
  PINS(w1s); PINS(stg);
  auto w1_issue = [&](auto K_, auto LC_) {
    dma16x4<8 * F * 2, LC_.value>(stg, w1E, w1O, rs_w1, w1s + K_.value * 32 * (F * 2));
  };
  auto w1_piece = [&](auto K_, auto P_, auto LC_) {
    constexpr int p = P_.value;
    SCHED();
    dma16c<LC_.value + p * 1024, (K_.value * 32 + (p >> 1) * 16) * (F * 2) + (p & 1) * 1024>(stg, (p & 1) ? w1O : w1E, rs_w1, w1s);
    SCHED();
  };
  constexpr u32 SC = 65536, SA = 98304, SB = 131072;

  asm volatile("s_waitcnt lgkmcnt(0)" : "+s"(epoch));
  u64 tag = (epoch + 1) * 0x9E3779B97F4A7C15ull + 0x632BE59BD9B4E019ull;
  PINS(tag);
  f32x4 aQ[2], aK[3], aV[3];
  const f32x4 z4 = {0, 0, 0, 0};
  u32x4 xres;
  float mL[3], fq[2][2], rk[2][2];
  f32x4 mR[3];

  auto p1_loop = [&](auto H_) {
    constexpr bool H = H_.value;
    u32x4 Xf[2][3], Wf[2][3], Sf[2];
    f32x4 sA = z4, aC = z4;
#pragma unroll
    for (int i = 0; i < 2; ++i) aQ[i] = z4;
#pragma unroll
    for (int i = 0; i < 3; ++i) { aK[i] = z4; aV[i] = z4; }
    const u32 xl = L_X + (r16 >> 2) * 4096 + (r16 & 3) * 256 + ((q ^ r16) << 4);
    u32 xb[4], xst[4], wr = trN ^ (hh << 5), wr2 = wr + 32768;
#pragma unroll
    for (int i = 0; i < 4; ++i) { xb[i] = (xl ^ (i << 6)) + (H ? 0 : 32768); PINV(xb[i]); }
    PINV(wr); PINV(wr2);
    u32 wov = (hh * 8 + (lane >> 3)) * 1024 + swzW;
    const u32 stok = min(10 * w + 3 * qp + pp, 79u), srow = pp == 3 ? 0u : stok;
    u32 fin = L_MR + stok * 4;
#pragma unroll
    for (int i = 0; i < 4; ++i) {
      xst[i] = L_X + (srow >> 2) * 4096 + (srow & 3) * 256 + (((q + 4 * i) ^ (srow & 15)) << 4);
      PINV(xst[i]);
    }
    PINV(fin);
    if constexpr (!H) PINV(wov);
    const bool toucher = c != cs;
    u32 tv1, tv2;
    {
      const u32 n = ((hh * 56 + tl * 7 + (c > cs ? c - 1 : c)) * 64 + lane);
      const u32 r1 = n / 7, r2 = n >> 3;
      tv1 = (((r1 >> 5) & 7) * 256 + (r1 >> 8) * 32 + (r1 & 31)) * (F * 2) + (n - r1 * 7) * 128 + sq * 896;
      tv2 = (((r2 >> 5) & 7) * 224 + (r2 >> 8) * 32 + (r2 & 31)) * (S * 2) + (n & 7) * 128 + sq * 1024;
      if constexpr (H) { PINV(tv1); PINV(tv2); }
    }
    u32x2 wh;
    float snm, srv;
    auto rd = [&](auto HS_, auto N_) {
      constexpr int hs = HS_.value, s = hs / 2, kk = hs & 1, n = N_.value;
      constexpr u32 so = L_RING + ((s + 1) % 3) * 24576 + kk * 12288, xo = (hs >> 2) * 1024;
      if constexpr (n == 0) Xf[kk][1] = LD(u32x4, xb[hs & 3] + xo + 16384);
      if constexpr (n >= 1 && n <= 6) {
        constexpr int seg = (n - 1) / 2;
        constexpr u32 o = so + seg * 4096 + ((n - 1) & 1) * 512;
        const u32x2 v = o < 65536 ? trd(wr + o) : trd(wr2 + (o - 32768));
        if constexpr (n & 1) wh = v;
        else Wf[kk][seg] = u32x4{wh.x, wh.y, v.x, v.y};
      }
      if constexpr (n == 7) Xf[kk][2] = LD(u32x4, xb[hs & 3] + xo + 32768);
      if constexpr (n == 8) Xf[kk][0] = LD(u32x4, xb[hs & 3] + xo);
    };
    auto mm = [&](auto KK_, auto N_) {
      constexpr int kk = KK_.value, n = N_.value;
      if constexpr (n == 0) aQ[0] = mfma32(Wf[kk][0], Xf[kk][1], aQ[0]);
      if constexpr (n == 1) aK[1] = mfma32(Wf[kk][1], Xf[kk][1], aK[1]);
      if constexpr (n == 2) aV[1] = mfma32(Xf[kk][1], Wf[kk][2], aV[1]);
      if constexpr (n == 3) aQ[1] = mfma32(Wf[kk][0], Xf[kk][2], aQ[1]);
      if constexpr (n == 4) aK[2] = mfma32(Wf[kk][1], Xf[kk][2], aK[2]);
      if constexpr (n == 5) aV[2] = mfma32(Xf[kk][2], Wf[kk][2], aV[2]);
      if constexpr (H && n == 6) aK[0] = mfma32(Wf[kk][1], Xf[kk][0], aK[0]);
      if constexpr (H && n == 7) aV[0] = mfma32(Xf[kk][0], Wf[kk][2], aV[0]);
      if constexpr (!H && n == 6) aC = mfma32(Wf[kk][0], ones, aC);
    };
    auto handover = [&]() {
    if constexpr (H) {
      LD(f32x4, exa) = aK[2];
      LD(f32x4, exa + 1024) = aV[2];
      if (r16 == 0) LD(f32x4, csa + 64) = aK[0];
      if (q == 0) LD(float, csv) = aV[0].x;
    } else {
      if (r16 == 0) LD(f32x4, csa) = aC;
    }
    };
    auto stream = [&](auto B_, auto I_) {
      constexpr int B = B_.value, i = I_.value;
      if constexpr (B <= 5 && i < 6) dma_w(IC(B + 2), I_);
      else if constexpr (B <= 3 && i < ((B & 1) ? 8 : 9)) dma_x(IC(i - 6 + (B & 1) * 3), IC(2 + B / 2));
      else if constexpr (B >= 6 && i < (B == 8 ? 4 : 6)) {
        constexpr int p = (B - 6) * 6 + i;
        dma16c<L_RING + p * 4096, p * 32768>(hh * 1024, wov, rs_wo, c * 128);
      }
    };
    SCHED();
    if constexpr (H) {
      sfor<5>([&](auto a) { dma_x(a, IC(1)); });
      if (w == 4) dma4(L_B1, (lane & 31) * 4, __builtin_amdgcn_make_buffer_rsrc((void*)b1, 0, F * 2, 0x00020000), j * 128);
      if (w == 5) dma4(L_B2, (lane & 31) * 4, __builtin_amdgcn_make_buffer_rsrc((void*)b2, 0, S * 2, 0x00020000), t * 128);
      if (w == 6) st8_sc1(u32x2{~0u, ~0u}, rs_ws, lane * 8, WS_ST + g * 512);
      if (w == 7 && lane < 12) {
        u64 zero = 0;
        PINV(zero);
        u32 mo = lane == 8 ? WS_F1 + g * 128 : (lane < 4 ? WS_F2 : WS_F3 - 256) + gr * 256 + lane * 64;
        if (lane == 9) mo = WS_O + (t * 8 + c) * 8192;
        if (lane == 10) mo = WS_HN + c * 262144 + t * 8192;
        if (lane == 11) mo = WS_U + c * (56 * 4096) + j * 8192 + (p3 ? 0 : 0x1c0000 - c * (56 * 4096));
        (void)__hip_atomic_fetch_add((GAS u64*)(ws + mo), zero, __ATOMIC_RELAXED, __HIP_MEMORY_SCOPE_AGENT);
      }
    } else {
      sfor<6>([&](auto i) { dma_w(IC(1), i); });
    }
    wait_vm<(H ? 7 : 6)>();
    BARRIER();
    sfor<(H ? 9 : 8)>([&](auto n) { rd(IC(0), n); });
    SCHED();
    sfor<16>([&](auto HS_) {
      constexpr int hs = HS_.value, s = hs / 2, kk = hs & 1;
      constexpr int f0 = stat_f0(hs), f1 = stat_f0(hs + 1);
      sfor<(H ? 8 : 7)>([&](auto N_) {
        constexpr int n = N_.value;
        mm(IC(kk), N_);
        SCHED();
        if constexpr (n < f1 - f0) Sf[n] = LD(u32x4, xst[(f0 + n) & 3] + ((f0 + n) >> 2) * 1024);
        if constexpr (H) {
          if constexpr (hs < 15) {
            rd(IC(hs + 1), N_);
            if constexpr (n == 7) rd(IC(hs + 1), IC(8));
          }
        } else {
          if constexpr (hs < 15) {
            if constexpr (n == 0) rd(IC(hs + 1), IC(0));
            rd(IC(hs + 1), IC(n + 1));
          }
          constexpr int B = (hs + 1) / 2;
          if constexpr (hs == 0) {
            if constexpr (n < 6) stream(IC(0), N_);
            if constexpr (n >= 4) stream(IC(0), IC(n + 2));
          } else if constexpr (hs == 15) {
            stream(IC(B), N_);
          } else {
            if constexpr (kk == 1 && n < 6) stream(IC(B), N_);
            if constexpr (kk == 1 && n == 6) stream(IC(B), IC(6));
            if constexpr (kk == 0 && n < 2) stream(IC(B), IC(7 + n));
          }
        }
        if constexpr (H && kk == 0 && n == 2 && s >= 2 && s < 2 + 6) {
          constexpr int i = s - 2 + 0;
          if (hh == (i & 3) && toucher) dma4(L_SCR, i < 4 ? tv1 : tv2, i < 4 ? rs_w1t : rs_w2, 0);
        }
        if constexpr (H && hs == 6 && n == 2) {
          if (w == 4 && !toucher) dma4(L_SCR, lane * 1024, rs_wo, tl * 65536 + ((3u - ((u32)(size_t)wo >> 7)) & 7u) * 128);
        }
        if constexpr (hs == 13 && n == 3) xres = LD(u32x4, xra);
        if constexpr (hs == 13 && n == 2) {
          float d0 = sA.x, d1 = sA.y, d2 = sA.z;
          PINV(d0); PINV(d1); PINV(d2);
          snm = mul1(sA.w, -1.0f / D);
          srv = mul1(pp == 0 ? d0 : pp == 1 ? d1 : d2, 1.0f / D);
        }
        if constexpr (hs == 13 && n == 4) srv = fmaxf(srv - snm * snm, 0.0f) + EPS;
        if constexpr (hs == 13 && n == 5) srv = __builtin_amdgcn_rsqf(srv);
        if constexpr (hs == 14 && n == 0) {
          if (q == qp && pp != 3) { LD(float, fin) = snm; LD(float, fin + 384) = srv; }
        }
        if constexpr (hs == 15) {
          if constexpr (n == 0) {
#pragma unroll
            for (int a = 0; a < 2; ++a) {
              fq[a][0] = LD(float, mrL + 384 + (a + 1) * 64);
              rk[a][0] = LD(float, rk0a + a * 64);
              rk[a][1] = LD(float, rk1a + a * 64);
            }
          }
          if constexpr (n == 1) {
#pragma unroll
            for (int a = 0; a < 3; ++a) { mL[a] = LD(float, mrL + a * 64); mR[a] = LD(f32x4, mrR + a * 64); }
          }
          if constexpr (n == 4) {
#pragma unroll
            for (int a = 0; a < 2; ++a) {
              const float cq = mul1(fq[a][0], 0.25f * 1.44269504088896f);
              fq[a][0] = mul1(rk[a][0], cq);
              fq[a][1] = mul1(rk[a][1], cq);
            }
          }
        }
        SCHED();
      });
      sfor<f1 - f0>([&](auto I_) {
        sA = mfma32(Sf[I_.value], Sf[I_.value], sA);
        SCHED();
      });
      if constexpr (kk == 0) {
        constexpr int B = s + 1;
        if constexpr (H) {
          if constexpr (B == 2) wait_vm<0>();
        } else {
          if constexpr (B < 8) wait_vm<(B == 2 ? 8 : B == 1 || B == 3 ? 9 : B == 4 ? 8 : 6)>();
        }
        barrier_lgkm<(hs == 14 ? 0 : H ? 2 : 1)>();
        SCHED();
      }
    });
    handover();
    BARRIER();
    if constexpr (!H) {
      aK[0] = LD(f32x4, exa);
      aV[0] = LD(f32x4, exa + 1024);
    }
  };
  if (heavy) p1_loop(std::true_type{}); else p1_loop(std::false_type{});
  constexpr int NREG = 2;
  u32x4 B1[8][4], B2[7][4], Bo[4][4];
  u32x4 hreg;
  auto bo_read = [&](int k) {
#pragma unroll
    for (int n = 0; n < 4; ++n) Bo[k][n] = trfrag(trO[n] + k * 4096);
  };
  float bias;
  u32x4 bb;
  {
    const f32x4 csQ = LD(f32x4, csa), csK = LD(f32x4, csa + 64);
    const float csV = LD(float, csv);
    bias = blo((u32)LD(u16, L_B2 + s_ * 2));
    bb = LD(u32x4, L_B1 + (w & 1) * 64 + q * 16);
    w1_piece(IC(0), IC(0), IC(SA));
    u32x2 qf[2], kf[3], vf[3];
#pragma unroll
    for (int a = 0; a < 2; ++a) {
      const f32x4 v = aQ[a] + mL[a + 1] * csQ;
      qf[a] = u32x2{pk(v[0], v[1]), pk(v[2], v[3])};
    }
    w1_piece(IC(0), IC(1), IC(SA));
#pragma unroll
    for (int a = 0; a < 3; ++a) {
      const f32x4 v = aK[a] + mL[a] * csK;
      kf[a] = u32x2{pk(v[0], v[1]), pk(v[2], v[3])};
    }
    w1_piece(IC(0), IC(2), IC(SA));
#pragma unroll
    for (int a = 0; a < 3; ++a) {
      const f32x4 v = aV[a] + mR[a] * csV;
      vf[a] = u32x2{pk(v[0], v[1]), pk(v[2], v[3])};
    }
    SCHED();
    f32x4 sp[2], so[2];
#pragma unroll
    for (int a = 0; a < 2; ++a) {
      sp[a] = mfma16(kf[a], qf[a], z4);
      so[a] = mfma16(kf[a + 1], qf[a], z4);
    }
    w1_piece(IC(0), IC(3), IC(SA));
    float lg[2][2], mx[2];
#pragma unroll
    for (int a = 0; a < 2; ++a) {
      const float ev0 = own0 ? so[a].x : sp[a].x, od0 = own0 ? so[a].y : sp[a].y;
      const float ev1 = own1 ? so[a].z : sp[a].z, od1 = own1 ? so[a].w : sp[a].w;
      lg[a][0] = (odd ? od0 : ev0) * fq[a][0];
      lg[a][1] = (odd ? od1 : ev1) * fq[a][1];
      if (a == 0) {
        const bool first = (t == 0) && heavy;
        lg[a][0] = (first && !own0) ? -1.44269504088896e9f : lg[a][0];
        lg[a][1] = (first && !own1) ? -1.44269504088896e9f : lg[a][1];
      }
      mx[a] = vmax(lg[a][0], lg[a][1]);
    }
    w1_piece(IC(1), IC(0), IC(SB));
    allred2<true>(mx[0], mx[1]);
    float ee[2][2], sm[2];
#pragma unroll
    for (int a = 0; a < 2; ++a) {
      ee[a][0] = __builtin_amdgcn_exp2f(lg[a][0] - mx[a]);
      ee[a][1] = __builtin_amdgcn_exp2f(lg[a][1] - mx[a]);
      sm[a] = ee[a][0] + ee[a][1];
    }
    w1_piece(IC(1), IC(1), IC(SB));
    allred2<false>(sm[0], sm[1]);
    u32x4 pf[2];
#pragma unroll
    for (int a = 0; a < 2; ++a) {
      const float inv = __builtin_amdgcn_rcpf(sm[a]);
      const u32 pw = pk(ee[a][0] * rk[a][0] * inv, ee[a][1] * rk[a][1] * inv);
      pf[a] = u32x4{__builtin_amdgcn_perm(0u, pw, sel0), __builtin_amdgcn_perm(0u, pw, sel1), __builtin_amdgcn_perm(0u, pw, sel2), __builtin_amdgcn_perm(0u, pw, sel3)};
      if (a == 0) w1_piece(IC(1), IC(2), IC(SB));
    }
    f32x4 o[2];
#pragma unroll
    for (int a = 0; a < 2; ++a) o[a] = mfma32(u32x4{vf[a].x, vf[a].y, vf[a + 1].x, vf[a + 1].y}, pf[a], z4);
    w1_piece(IC(1), IC(3), IC(SB));
    auto s0 = __builtin_amdgcn_permlane16_swap(pk(o[0][0], o[0][1]), pk(o[1][0], o[1][1]), false, false);
    auto s1 = __builtin_amdgcn_permlane16_swap(pk(o[0][2], o[0][3]), pk(o[1][2], o[1][3]), false, false);
    const u32x4 ov = {s0[0], s1[0], s0[1], s1[1]};
    st_sc1(ov, rs_ws, ooff, WS_O + (t * 8 + c) * 8192 + ta * 4096 + hh * 512);
  }
  wait_vm<9>();
  BARRIER();
  bo_read(0); bo_read(1);
  w1_issue(IC(2), IC(SC));
  bo_read(2); bo_read(3);
  u32 trS[4], lane16 = lane * 16;
#pragma unroll
  for (int n = 0; n < 4; ++n) { trS[n] = (trN ^ (n << 5)) + stg + SA; PINV(trS[n]); }
  auto rd4 = [&](u32x4(&B)[4], u32 slot) {
#pragma unroll
    for (int n = 0; n < 4; ++n) {
      u32 a = trS[n] + (slot - SA);
      if (slot < SA) PINV(a);
      B[n] = trfrag(a);
    }
  };
  auto lgkm0 = [&]() { SCHED(); WAIT_LGKM0(); SCHED(); };
  u32 w2E = (lane >> 3) * (S * 2) + swzE, w2O = (lane >> 3) * (S * 2) + (swzE ^ 64) + 8 * (S * 2) - 1024;
  const u32 w2s = w * 224 * (S * 2) + t * 128;
  auto w2_issue = [&](auto K_, auto LC_) { dma16x4<8 * S * 2, LC_.value>(stg, w2E, w2O, rs_w2, w2s + K_.value * 32 * (S * 2)); };
  u32 redw = q * 256 + ((r16 ^ ((q >> 1) << 1)) << 4);
  u32 redr = (((gq >> 1) * 4 + (s_ >> 4)) * 1024) + ((gq & 1) * 32 + ((s_ & 15) ^ (gq << 1))) * 16;
  PINV(redw); PINV(redr); PINV(lane16);
  u64 tagv = tag;
  u32 fo = WS_F1 + (t * 8 + lane) * 128 + c * 8;
  PINV(tagv); PINV(fo);
  wait_vm<4>();
  BARRIER();
  if (tid < 8) put8(ws, fo, tagv);
  {
    rd4(B1[0], SA); rd4(B1[1], SB);
    lgkm0();
    w1_issue(IC(3), IC(SA));
    poll_tag<2>(ws + (WS_F1 + g * 128 + hh * 16), lane, tag);
    u32x4 ofr[8];
    {
      const u32 os0 = WS_O + (t * 8 + 2 * hh) * 8192 + th * 4096, os1 = os0 + 8192;
      asm volatile(
          "buffer_load_dwordx4 %0, %8, %9, %10 offen sc1\n\t"
          "buffer_load_dwordx4 %1, %8, %9, %10 offen offset:1024 sc1\n\t"
          "buffer_load_dwordx4 %2, %8, %9, %11 offen sc1\n\t"
          "buffer_load_dwordx4 %3, %8, %9, %11 offen offset:1024 sc1\n\t"
          "buffer_load_dwordx4 %4, %8, %9, %10 offen offset:2048 sc1\n\t"
          "buffer_load_dwordx4 %5, %8, %9, %10 offen offset:3072 sc1\n\t"
          "buffer_load_dwordx4 %6, %8, %9, %11 offen offset:2048 sc1\n\t"
          "buffer_load_dwordx4 %7, %8, %9, %11 offen offset:3072 sc1"
          : "=&v"(ofr[0]), "=&v"(ofr[1]), "=&v"(ofr[2]), "=&v"(ofr[3]), "=&v"(ofr[4]), "=&v"(ofr[5]), "=&v"(ofr[6]), "=&v"(ofr[7])
          : "v"(lane16), "s"(rs_ws), "s"(os0), "s"(os1)
          : "memory");
    }
    rd4(B1[2], SC); rd4(B1[3], SA);
    f32x4 xr0 = {blo(xres.x), bhi(xres.x), blo(xres.y), bhi(xres.y)}, xr1 = {blo(xres.z), bhi(xres.z), blo(xres.w), bhi(xres.w)};
    PINV(xr0); PINV(xr1);
    SCHED();
    sfor<2>([&](auto TT_) {
      constexpr int tt = TT_.value;
      f32x4 acc[4];
#pragma unroll
      for (int n = 0; n < 4; ++n) acc[n] = z4;
      sfor<4>([&](auto K_) {
        constexpr int k = K_.value, i = tt * 4 + k;
        wait_tie<7 - i + i / 2>(ofr[i]);
#pragma unroll
        for (int n = 0; n < 4; ++n) acc[n] = mfma32(Bo[k][n], ofr[i], acc[n]);
        if constexpr (i & 1) w1_piece(IC(4), IC(i / 2), IC(SB));
        SCHED();
      });
#pragma unroll
      for (int n = 0; n < 4; ++n) LD(f32x4, L_RING + hh * 16384 + th * 2048 + ((redw ^ (n << 6)) + (n * 4 + tt) * 1024)) = acc[n];
    });
    BARRIER();
    f32x4 pr[8];
#pragma unroll
    for (int k = 0; k < 4; ++k) {
      pr[2 * k] = LD(f32x4, L_RING + k * 16384 + redr);
      pr[2 * k + 1] = LD(f32x4, L_RING + k * 16384 + redr + 256);
    }
    w1_piece(IC(5), IC(0), IC(SC));
    const f32x4 a0 = (pr[0] + pr[2]) + (pr[4] + pr[6]), a1 = (pr[1] + pr[3]) + (pr[5] + pr[7]);
    w1_piece(IC(5), IC(1), IC(SC));
    hreg = u32x4{pk(xr0[0] + a0[0], xr0[1] + a0[1]), pk(xr0[2] + a0[2], xr0[3] + a0[3]), pk(xr1[0] + a1[0], xr1[1] + a1[1]), pk(xr1[2] + a1[2], xr1[3] + a1[3])};
    w1_piece(IC(5), IC(2), IC(SC));
    const float ps = sum8(dot2(hreg.x, 0x3f803f80u, dot2(hreg.y, 0x3f803f80u, 0.0f)) + dot2(hreg.z, 0x3f803f80u, dot2(hreg.w, 0x3f803f80u, 0.0f)));
    const float pq = sum8(dot2(hreg.x, hreg.x, dot2(hreg.y, hreg.y, 0.0f)) + dot2(hreg.z, hreg.z, dot2(hreg.w, hreg.w, 0.0f)));
    if (gq == 0) st8_sc1(u32x2{min(f2u(ps), 0xfffffffeu), min(f2u(pq), 0xfffffffeu)}, rs_ws, s_ * 8, WS_ST + g * 512);
    BARRIER();
    w1_piece(IC(5), IC(3), IC(SC));
    w1_issue(IC(6), IC(SA));
    wait_vm<9>();
    rd4(B1[4], SB);
    lgkm0();
    w1_issue(IC(7), IC(SB));
    f32x4 h0 = {blo(hreg.x), bhi(hreg.x), blo(hreg.y), bhi(hreg.y)}, h1 = {blo(hreg.z), bhi(hreg.z), blo(hreg.w), bhi(hreg.w)};
    u32 tw = w * 1024 + (lane16 ^ ((q & 1) << 6)), tr = w * 1024 + qp * 128 + ((q * 32) ^ ((qp >> 1) << 6)) + pp * 8, hna = q * 1024 + r16 * 16;
    u32 hns = WS_HN + c * 262144 + (2 * t + th) * 4096 + hh * 256;
    PINV(h0); PINV(h1); PINV(tw); PINV(tr); PINV(hna); PINS(hns); PINV(w2E); PINV(w2O);
    __builtin_amdgcn_s_sleep(6);
    u32x2 sv;
    for (int sp = 0; sp < SPIN_CAP; ++sp) {
      FENCE();
      sv = __builtin_amdgcn_raw_buffer_load_b64(rs_ws, gq * 512 + s_ * 8, WS_ST + t * 4096, 16);
      if (!__any(max(sv.x, sv.y) == 0xffffffffu)) break;
    }
    {
      const float m = sum8(u2f(sv.x)) * (1.0f / D);
      const float v = fmaxf(sum8(u2f(sv.y)) * (1.0f / D) - m * m, 0.0f);
      const float r = __builtin_amdgcn_rsqf(v * (1.0f + EPS) + EPS * EPS);
      const float mr = -m * r;
      const f32x4 n0 = h0 * r + mr, n1 = h1 * r + mr;
      LD(u32x4, tw) = u32x4{pk(n0[0], n0[1]), pk(n0[2], n0[3]), pk(n1[0], n1[1]), pk(n1[2], n1[3])};
      FENCE();
      st_sc1(trfrag(tr), rs_ws, hna, hns);
    }
    SCHED();
    rd4(B1[5], SC);
    rd4(B1[6], SA);
    rd4(B1[7], SB);
    BARRIER();
    w2_issue(IC(0), IC(SC));
    fo = WS_F2 + c * 256 + t * 8 + lane * 2048;
    PINV(fo);
    wait_vm<4>();
    BARRIER();
    if (tid < NREP) put8(ws, fo, tagv);
  }

  w2_issue(IC(1), IC(SA));
  w2_issue(IC(NREG), IC(SB));
  if (p3) {
    const u32 mte = w >> 1, kp = w & 1;
    poll_tag<4>(ws + (WS_F2 + gr * 256 + w * 32), lane, tag);
    constexpr int RING = 7;
    f32x4 s0, s1;
    u32 ra, us;
    u32x4 A[RING];
    const u32 as = WS_HN + c * 262144 + w * 32768;
#pragma unroll
    for (int i = 0; i < RING; ++i) { A[i] = ld_sc1(rs_ws, lane16 + (i >> 3) * 1024, as + (i & 7) * 4096); SCHED(); }
    SCHED();
    rd4(B2[0], SC);
    rd4(B2[1], SA);
    SCHED();
#pragma unroll
    for (int mt = 0; mt < 4; ++mt) {
      if (mt == 0) __builtin_amdgcn_s_setprio(3);
      if (mt == 1) __builtin_amdgcn_s_setprio(2);
      if (mt == 2) __builtin_amdgcn_s_setprio(1);
      if (mt == 3) __builtin_amdgcn_s_setprio(0);
      f32x4 acc[4];
#pragma unroll
      for (int n = 0; n < 4; ++n) acc[n] = z4;
#pragma unroll
      for (int k = 0; k < 8; ++k) {
        const int i = mt * 8 + k;
        const u32x4 cur = A[i % RING];
#pragma unroll
        for (int n = 0; n < 4; ++n) acc[n] = mfma32(B1[k][n], cur, acc[n]);
        if (i + RING < 32) A[i % RING] = ld_sc1(rs_ws, lane16 + ((i + RING) >> 3) * 1024, as + ((i + RING) & 7) * 4096);
        if (i == 27) rd4(B2[NREG], SB);
        if (i == 29) {
          s0 = f32x4{blo(bb.x), bhi(bb.x), blo(bb.y), bhi(bb.y)}; s1 = f32x4{blo(bb.z), bhi(bb.z), blo(bb.w), bhi(bb.w)};
          PINV(s0); PINV(s1);
        }
        if (i == 30) {
          ra = mte * 32768 + (2 * kp + (q >> 1)) * 1024 + ((q & 1) * 32 + r16) * 16;
          us = WS_U + c * (56 * 4096) + (2 * j + kp) * 4096 + mte * 1024;
          PINV(ra); PINS(us);
        }
        SCHED();
      }
#pragma unroll
      for (int n = 0; n < 4; ++n) LD(f32x4, stg + lane16 + mt * 32768 + n * 1024) = acc[n];
    }
    BARRIER();
    {
      red16(ra, s0, s1, [&]() { w2_issue(IC(NREG + 1), IC(SB)); });
      const f32x4 z0 = s0 * (s0 * s0 * (-0.044715f * 2.302208198f) - 2.302208198f), z1 = s1 * (s1 * s1 * (-0.044715f * 2.302208198f) - 2.302208198f);
      f32x4 e0, e1;
#pragma unroll
      for (int e = 0; e < 4; ++e) { e0[e] = __builtin_amdgcn_exp2f(z0[e]); e1[e] = __builtin_amdgcn_exp2f(z1[e]); }
      e0 += 1.0f; e1 += 1.0f;
#pragma unroll
      for (int e = 0; e < 4; ++e) { e0[e] = __builtin_amdgcn_rcpf(e0[e]); e1[e] = __builtin_amdgcn_rcpf(e1[e]); }
      s0 *= e0; s1 *= e1;
      st_sc1(u32x4{pk(s0[0], s0[1]), pk(s0[2], s0[3]), pk(s1[0], s1[1]), pk(s1[2], s1[3])}, rs_ws, lane16, us);
    }
    BARRIER();
    w2_issue(IC(NREG + 2), IC(SA));
    fo = WS_F3 + c * 256 + j * 8 + lane * 2048;
    PINV(fo);
    if (j == 0 && c == 0 && tid == 0) put8(ws, WS_EPOCH, epoch + 1);
    wait_vm<4>();
    BARRIER();
    if (tid < NREP) put8(ws, fo, tagv);
  } else {
#pragma unroll
    for (int k = 0; k < 2; ++k)
      dma4(32768 + stg, (lane >> 3) * (S * 2) + (lane & 7) * 128, rs_w2, (w * 224 + 96 + k * 64 + c * 8) * (S * 2) + sq * 1024);
    wait_vm<0>();
    rd4(B2[0], SC);
    rd4(B2[1], SA);
    rd4(B2[NREG], SB);
    lgkm0();
    w2_issue(IC(NREG + 1), IC(SB));
    w2_issue(IC(NREG + 2), IC(SA));
    sfor<4 - NREG>([&](auto i) { w2_issue(IC(NREG + 3 + i.value), IC(i.value * 32768)); });
  }

  {
    constexpr int NR = 4 - NREG;
    if (p3) sfor<NR>([&](auto i) { w2_issue(IC(NREG + 3 + i.value), IC(i.value * 32768)); });
    poll_tag<4>(ws + (WS_F3 + gr * 256 + ((7 * w) >> 1) * 8), lane, tag);
    constexpr int RING = 8;
    f32x4 s0, s1;
    u32 redh, oa;
    u32x4 A[RING];
    const u32 as = WS_U + c * (56 * 4096) + w * (7 * 4096);
#pragma unroll
    for (int i = 0; i < RING; ++i) { A[i] = ld_sc1(rs_ws, lane16 + (i / 7) * 1024, as + (i % 7) * 4096); SCHED(); }
    SCHED();
    s0 = f32x4{blo(hreg.x) + bias, bhi(hreg.x) + bias, blo(hreg.y) + bias, bhi(hreg.y) + bias};
    s1 = f32x4{blo(hreg.z) + bias, bhi(hreg.z) + bias, blo(hreg.w) + bias, bhi(hreg.w) + bias};
    redh = redr + (gq >> 1) * 28672; oa = s_ * 1024 + gq * 16;
    PINV(s0); PINV(s1); PINV(redh); PINV(oa);
    rd4(B2[NREG + 1], SB);
    rd4(B2[NREG + 2], SA);
    sfor<NR>([&](auto i) { rd4(B2[NREG + 3 + i.value], i.value * 32768); });
    SCHED();
#pragma unroll
    for (int mt = 0; mt < 4; ++mt) {
      if (mt == 0) __builtin_amdgcn_s_setprio(3);
      if (mt == 1) __builtin_amdgcn_s_setprio(2);
      if (mt == 2) __builtin_amdgcn_s_setprio(1);
      if (mt == 3) __builtin_amdgcn_s_setprio(0);
      f32x4 acc[4];
#pragma unroll
      for (int n = 0; n < 4; ++n) acc[n] = z4;
#pragma unroll
      for (int k = 0; k < 7; ++k) {
        const int i = mt * 7 + k;
        const u32x4 cur = A[i % RING];
#pragma unroll
        for (int n = 0; n < 4; ++n) acc[n] = mfma32(cur, B2[k][n], acc[n]);
        if (i + RING < 28) A[i % RING] = ld_sc1(rs_ws, lane16 + ((i + RING) / 7) * 1024, as + ((i + RING) % 7) * 4096);
        SCHED();
      }
#pragma unroll
      for (int n = 0; n < 4; ++n) LD(f32x4, stg + (redw ^ (mt << 6)) + mt * 32768 + n * 1024) = acc[n];
    }
    BARRIER();
    red16(redh, s0, s1, []() {});
    const u32x4 ov = {pk(s0[0], s0[1]), pk(s0[2], s0[3]), pk(s1[0], s1[1]), pk(s1[2], s1[3])};
    __builtin_amdgcn_raw_buffer_store_b128(ov, rs_out, oa, t * 65536 + c * 128, 16);
  }
  asm volatile(".Lfb_end_mark:" ::: "memory");
}

extern "C" void kernel_launch(void* const* d_in, const int* in_sizes, int n_in, void* d_out, int out_size, void* d_ws, size_t ws_size,
                              hipStream_t stream) {
  (void)in_sizes; (void)n_in; (void)out_size; (void)ws_size;
  (void)hipFuncSetAttribute((const void*)fused_block, hipFuncAttributeMaxDynamicSharedMemorySize, L_TOTAL);
  fused_block<<<256, 512, L_TOTAL, stream>>>((const u16*)d_in[0], (const u16*)d_in[1], (char*)d_ws, (const u16*)d_in[2], (const u16*)d_in[4],
                                             (const u16*)d_in[6], (const u16*)d_in[5], (const u16*)d_in[7], (u16*)d_out);
}
```
